```python
import torch

def _softmax(x, axis=-1):
    return torch.softmax(x, dim=axis)
def _gelu(x):
    return torch.nn.functional.gelu(x, approximate="tanh")
def _silu(x):
    return torch.nn.functional.silu(x)
def _sigmoid(x):
    return torch.sigmoid(x)
def _layernorm(x, axis=-1, eps=1e-5):
    m = x.mean(dim=axis, keepdim=True)
    v = x.var(dim=axis, keepdim=True, unbiased=False)
    return (x - m) / torch.sqrt(v + eps)
def _scatter_add(tgt, idx, src, axis=0):
    return tgt.clone().index_add_(axis, idx, src)
def _segment_sum(src, seg, n):
    out = torch.zeros((n,) + src.shape[1:], dtype=src.dtype)
    return out.index_add_(0, seg, src)
def _pad(x, pw):
    flat = tuple(v for pair in reversed(pw) for v in pair)
    return torch.nn.functional.pad(x, flat)

kernel_name = 'concat_heads__gqa_block_cached__mha_block_cached__near_ridge'

def setup_inputs():

    _w = (lambda a: (lambda t: t.to(torch.bfloat16) if t.is_floating_point() else t)(torch.as_tensor(a)))
    def _A_setup():
        B, Sq, D = 16, 64, 512
        Hq, Hkv, Sk, Dh = 32, 32, 64, 128
        assert Hq % Hkv == 0, (Hq, Hkv)
        return {
            "x": _w((lambda *s: torch.randn(*s, dtype=torch.bfloat16) * 0.8)(B, Sq, D)),
            "k_cache": _w((lambda *s: torch.randn(*s, dtype=torch.bfloat16) * 0.8)(B, Hkv, Sk, Dh)),
            "v_cache": _w((lambda *s: torch.randn(*s, dtype=torch.bfloat16) * 0.05)(B, Hkv, Sk, Dh)),
            "wq": _w((lambda *s: torch.randn(*s, dtype=torch.bfloat16) * 0.8)(D, Hq * Dh) * (D ** -0.5)),
            "wo": _w((lambda *s: torch.randn(*s, dtype=torch.bfloat16) * 0.05)(Hq * Dh, D) * ((Hq * Dh) ** -0.5)),
        }
    _A_in = _A_setup()

    def _B_setup():
        B, Sq, D = 16, 64, 512
        H, Sk, Dh = 32, 64, 128
        return {
            "x": _w((lambda *s: torch.randn(*s, dtype=torch.bfloat16) * 0.8)(B, Sq, D)),
            "k_cache": _w((lambda *s: torch.randn(*s, dtype=torch.bfloat16) * 0.8)(B, H, Sk, Dh)),
            "v_cache": _w((lambda *s: torch.randn(*s, dtype=torch.bfloat16) * 0.05)(B, H, Sk, Dh)),
            "wq": _w((lambda *s: torch.randn(*s, dtype=torch.bfloat16) * 0.8)(D, H * Dh) * (D ** -0.5)),
            "wo": _w((lambda *s: torch.randn(*s, dtype=torch.bfloat16) * 0.05)(H * Dh, D) * ((H * Dh) ** -0.5)),
        }
    _B_in = _B_setup()

    return {
        "A_x": _A_in["x"],
        "A_k_cache": _A_in["k_cache"],
        "A_v_cache": _A_in["v_cache"],
        "A_wq": _A_in["wq"],
        "A_wo": _A_in["wo"],
        "B_x": _B_in["x"],
        "B_k_cache": _B_in["k_cache"],
        "B_v_cache": _B_in["v_cache"],
        "B_wq": _B_in["wq"],
        "B_wo": _B_in["wo"],
    }

def reference(A_x, A_k_cache, A_v_cache, A_wq, A_wo, B_x, B_k_cache, B_v_cache, B_wq, B_wo):
    def _A_ref(x, k_cache, v_cache, wq, wo):
        B, Sq, D = x.shape
        Hq, Dh = 32, 128
        Hkv = k_cache.shape[1]
        rep = Hq // Hkv
        q = (lambda x, *d: x.permute(*d))((x @ wq).reshape(B, Sq, Hq, Dh), 0, 2, 1, 3)
        k = (lambda x, n, axis: torch.repeat_interleave(x, n, dim=axis))(k_cache, rep, axis=1)
        v = (lambda x, n, axis: torch.repeat_interleave(x, n, dim=axis))(v_cache, rep, axis=1)
        scores = torch.einsum("bhqd,bhkd->bhqk", q, k) * (Dh ** -0.5)
        p = _softmax(scores, axis=-1)
        o = torch.einsum("bhqk,bhkd->bhqd", p, v)
        return (lambda x, *d: x.permute(*d))(o, 0, 2, 1, 3).reshape(B, Sq, Hq * Dh) @ wo

    def _B_ref(x, k_cache, v_cache, wq, wo):
        B, Sq, D = x.shape
        H, Dh = 32, 128
        q = (lambda x, *d: x.permute(*d))((x @ wq).reshape(B, Sq, H, Dh), 0, 2, 1, 3)
        scores = torch.einsum("bhqd,bhkd->bhqk", q, k_cache) * (Dh ** -0.5)
        p = _softmax(scores, axis=-1)
        o = torch.einsum("bhqk,bhkd->bhqd", p, v_cache)
        return (lambda x, *d: x.permute(*d))(o, 0, 2, 1, 3).reshape(B, Sq, H * Dh) @ wo

    a = _A_ref(A_x, A_k_cache, A_v_cache, A_wq, A_wo)
    b = _B_ref(B_x, B_k_cache, B_v_cache, B_wq, B_wo)
    return torch.concatenate([a, b], axis=-1)
```

```cpp
#include <hip/hip_runtime.h>
#include <hip/hip_bf16.h>
struct KArgs { const char* in[10]; char* out; char* ws; unsigned ws_bytes; unsigned gap; };
__global__ void __launch_bounds__(512, 2) fused_attn_block(KArgs a) {
  asm volatile(
  "s_and_b32 s33, %[wg], 7\n"
  "s_lshr_b32 s75, s33, 2\n"
  "s_mul_i32 s36, s75, 40\n"
  "s_load_dwordx8 s[84:91], %[ka], s36\n"
  "s_getpc_b64 s[60:61]\n"
  "Lcode0:\n"
  "s_lshr_b32 s64, %[wg], 3\n"
  "s_lshl_b32 s64, s64, 13\n"
  "s_mov_b32 s62, Lcode1-Lcode0\n"
  "s_mov_b32 s63, 0x20000\n"
  "v_lshlrev_b32 v252, 4, v0\n"
  "s_and_b32 s61, s61, 0xffff\n"
  "buffer_load_dwordx4 v[248:251], v252, s[60:63], s64 offen\n"
  "s_add_u32 s74, s36, 32\n"
  "s_load_dwordx2 s[56:57], %[ka], s74\n"
  "s_load_dwordx4 s[96:99], %[ka], 0x50\n"
  "s_load_dword s26, %[ka], 0x60\n"
  "v_readfirstlane_b32 s95, v0\n"
  "s_bfe_u32 s34, %[wg], 0x20003\n"
  "s_lshr_b32 s35, %[wg], 5\n"
  "s_and_b32 s92, s33, 3\n"
  "s_lshl_b32 s37, s95, 6\n"
  "s_and_b32 s37, s37, 0x3000\n"
  "s_mov_b32 s27, 0x20000\n"
  "v_and_b32 v0, 63, v0\n"
  "s_cmp_ge_u32 s95, 256\n"
  "s_cbranch_scc0 Lcompute\n"
  "s_lshl_b32 s81, s92, 2\n"
  "s_add_u32 s81, s81, s34\n"
  "s_lshl_b32 s76, s35, 17\n"
  "s_lshl_b32 s77, s81, 9\n"
  "s_mov_b32 s7, 0x20000\n"
  "s_mov_b32 s11, 0x20000\n"
  "s_sub_u32 s6, 0x100000, s76\n"
  "s_sub_u32 s10, 0x400000, s77\n"
  "v_lshrrev_b32 v30, 4, v0\n"
  "v_and_b32 v31, 7, v0\n"
  "v_lshrrev_b32 v32, 3, v0\n"
  "v_xor_b32 v33, v31, v30\n"
  "v_lshlrev_b32 v33, 4, v33\n"
  "v_lshl_add_u32 v33, v32, 10, v33\n"
  "s_lshl_b32 s42, s37, 3\n"
  "v_add_u32 v2, s42, v33\n"
  "v_xor_b32 v3, 64, v2\n"
  "v_add_u32 v4, 14336, v2\n"
  "v_add_u32 v5, 21504, v3\n"
  "v_add_u32 v3, 7168, v3\n"
  "v_and_b32 v33, 1, v30\n"
  "v_lshlrev_b32 v33, 1, v33\n"
  "v_xor_b32 v33, v33, v31\n"
  "v_lshlrev_b32 v33, 4, v33\n"
  "v_lshl_add_u32 v33, v32, 13, v33\n"
  "s_bfe_u32 s42, s37, 0x1000c\n"
  "s_lshr_b32 s43, s37, 13\n"
  "s_lshl_b32 s44, s42, 18\n"
  "s_lshl_b32 s45, s43, 7\n"
  "s_add_u32 s44, s44, s45\n"
  "v_add_u32 v6, s44, v33\n"
  "v_xor_b32 v7, 64, v6\n"
  "v_add_u32 v8, 129024, v6\n"
  "v_add_u32 v9, 193536, v7\n"
  "v_add_u32 v7, 64512, v7\n"
  "s_waitcnt lgkmcnt(0)\n"
  "s_add_u32 s4, s84, s76\n"
  "s_addc_u32 s5, s85, 0\n"
  "s_and_b32 s5, s5, 0xffff\n"
  "s_add_u32 s8, s90, s77\n"
  "s_addc_u32 s9, s91, 0\n"
  "s_and_b32 s9, s9, 0xffff\n"
  "s_mov_b32 s91, 0\n"
  "s_add_u32 m0, s37, 65536\n"
  "s_nop 0\n"
  "buffer_load_dwordx4 v2, s[4:7], s91 offen lds\n"
  "buffer_load_dwordx4 v3, s[4:7], s91 offen offset:1024 lds\n"
  "buffer_load_dwordx4 v4, s[4:7], s91 offen offset:2048 lds\n"
  "buffer_load_dwordx4 v5, s[4:7], s91 offen offset:3072 lds\n"
  "s_mov_b32 s91, 0\n"
  "s_add_u32 m0, s37, 0\n"
  "s_nop 0\n"
  "buffer_load_dwordx4 v6, s[8:11], s91 offen lds\n"
  "buffer_load_dwordx4 v7, s[8:11], s91 offen offset:1024 lds\n"
  "buffer_load_dwordx4 v8, s[8:11], s91 offen offset:2048 lds\n"
  "buffer_load_dwordx4 v9, s[8:11], s91 offen offset:3072 lds\n"
  "s_mov_b32 s91, 0\n"
  "s_add_u32 s91, s91, 256\n"
  "s_add_u32 m0, s37, 16384\n"
  "s_nop 0\n"
  "buffer_load_dwordx4 v6, s[8:11], s91 offen lds\n"
  "buffer_load_dwordx4 v7, s[8:11], s91 offen offset:1024 lds\n"
  "buffer_load_dwordx4 v8, s[8:11], s91 offen offset:2048 lds\n"
  "buffer_load_dwordx4 v9, s[8:11], s91 offen offset:3072 lds\n"
  "s_lshl_b32 s39, s34, 1\n"
  "s_add_u32 s39, s39, s35\n"
  "s_cmp_ge_u32 s39, 7\n"
  "s_cselect_b32 s91, 7, 0\n"
  "s_sub_u32 s39, s39, s91\n"
  "s_cmp_ge_u32 s39, 7\n"
  "s_cselect_b32 s91, 7, 0\n"
  "s_sub_u32 s39, s39, s91\n"
  "s_add_u32 s69, s39, 1\n"
  "s_add_u32 s39, s39, 1\n"
  "s_cmp_ge_u32 s39, 7\n"
  "s_cselect_b32 s39, 0, s39\n"
  "s_add_u32 s70, s39, 1\n"
  "s_add_u32 s39, s39, 1\n"
  "s_cmp_ge_u32 s39, 7\n"
  "s_cselect_b32 s39, 0, s39\n"
  "s_add_u32 s71, s39, 1\n"
  "s_add_u32 s39, s39, 1\n"
  "s_cmp_ge_u32 s39, 7\n"
  "s_cselect_b32 s39, 0, s39\n"
  "s_add_u32 s72, s39, 1\n"
  "s_add_u32 s39, s39, 1\n"
  "s_cmp_ge_u32 s39, 7\n"
  "s_cselect_b32 s39, 0, s39\n"
  "s_add_u32 s73, s39, 1\n"
  "s_add_u32 s39, s39, 1\n"
  "s_cmp_ge_u32 s39, 7\n"
  "s_cselect_b32 s39, 0, s39\n"
  "s_add_u32 s74, s39, 1\n"
  "s_add_u32 s39, s39, 1\n"
  "s_cmp_ge_u32 s39, 7\n"
  "s_cselect_b32 s39, 0, s39\n"
  "s_add_u32 s75, s39, 1\n"
  "s_add_u32 s39, s39, 1\n"
  "s_cmp_ge_u32 s39, 7\n"
  "s_cselect_b32 s39, 0, s39\n"
  "s_lshl_b32 s78, s35, 5\n"
  "s_add_u32 s78, s78, s81\n"
  "s_lshl_b32 s78, s78, 15\n"
  "s_lshl_b32 s79, s92, 12\n"
  "s_add_u32 s79, s79, s34\n"
  "s_lshl_b32 s79, s79, 8\n"
  "s_mov_b32 s15, 0x20000\n"
  "s_mov_b32 s19, 0x20000\n"
  "s_mov_b32 s23, 0x20000\n"
  "s_sub_u32 s14, 0x800000, s78\n"
  "s_mov_b32 s18, s14\n"
  "s_sub_u32 s22, 0x400000, s79\n"
  "s_add_u32 s12, s86, s78\n"
  "s_addc_u32 s13, s87, 0\n"
  "s_and_b32 s13, s13, 0xffff\n"
  "s_add_u32 s16, s88, s78\n"
  "s_addc_u32 s17, s89, 0\n"
  "s_and_b32 s17, s17, 0xffff\n"
  "s_add_u32 s20, s56, s79\n"
  "s_addc_u32 s21, s57, 0\n"
  "s_and_b32 s21, s21, 0xffff\n"
  "s_mov_b32 s24, s98\n"
  "s_and_b32 s25, s99, 0xffff\n"
  "v_lshlrev_b32 v33, 1, v30\n"
  "v_xor_b32 v33, v33, v31\n"
  "v_lshlrev_b32 v33, 4, v33\n"
  "v_lshl_add_u32 v34, v32, 8, v33\n"
  "s_lshl_b32 s44, s42, 13\n"
  "s_add_u32 s44, s44, s45\n"
  "v_add_u32 v14, s44, v34\n"
  "v_add_u32 v15, 1024, v14\n"
  "v_add_u32 v16, 2048, v14\n"
  "v_add_u32 v17, 3072, v14\n"
  "v_and_b32 v34, 15, v0\n"
  "v_xor_b32 v34, v34, v30\n"
  "v_lshlrev_b32 v34, 4, v34\n"
  "v_lshl_add_u32 v34, v30, 8, v34\n"
  "v_add_u32 v18, s37, v34\n"
  "v_xor_b32 v19, 64, v18\n"
  "v_xor_b32 v20, 128, v18\n"
  "v_xor_b32 v21, 192, v18\n"
  "s_lshl_b32 s95, s33, 3\n"
  "s_add_u32 s95, s95, s35\n"
  "s_lshr_b32 s93, s37, 5\n"
  "s_lshl_b32 s78, s95, 9\n"
  "s_add_u32 s78, s78, s93\n"
  "s_add_u32 s78, s78, 25427968\n"
  "s_lshl_b32 s68, s95, 11\n"
  "s_add_u32 s68, s68, s93\n"
  "s_add_u32 s68, s68, 25165824\n"
  "s_lshl_b32 s94, s95, 18\n"
  "s_lshl_b32 s44, s37, 1\n"
  "s_add_u32 s94, s94, s44\n"
  "s_load_dwordx2 s[50:51], s[24:25], s78\n"
  "s_xor_b32 s39, s92, 3\n"
  "s_lshr_b32 s44, s39, 1\n"
  "s_xor_b32 s44, s44, s43\n"
  "s_lshl_b32 s44, s44, 7\n"
  "s_lshl_b32 s45, s42, 15\n"
  "s_add_u32 s44, s44, s45\n"
  "s_and_b32 s39, s39, 1\n"
  "s_lshl_b32 s39, s39, 6\n"
  "v_xor_b32 v33, s39, v33\n"
  "v_lshl_add_u32 v33, v32, 10, v33\n"
  "v_add_u32 v10, s44, v33\n"
  "v_add_u32 v11, 7168, v10\n"
  "v_add_u32 v12, 14336, v10\n"
  "v_add_u32 v13, 21504, v10\n"
  "v_lshlrev_b32 v22, 4, v0\n"
  "v_add_u32 v36, s37, v22\n"
  "v_min_u32 v23, 2, v0\n"
  "v_add_u32 v23, 1, v23\n"
  "v_add_u32 v23, s34, v23\n"
  "v_and_b32 v23, 3, v23\n"
  "v_lshlrev_b32 v23, 9, v23\n"
  "v_mov_b32 v28, 0\n"
  "s_lshl_b32 s66, s34, 18\n"
  "s_mov_b32 s39, s35\n"
  "s_cmp_ge_u32 s39, 6\n"
  "s_cselect_b32 s44, 6, 0\n"
  "s_sub_u32 s39, s39, s44\n"
  "s_lshr_b32 s44, s39, 1\n"
  "s_add_u32 s44, s44, s34\n"
  "s_add_u32 s44, s44, 1\n"
  "s_and_b32 s44, s44, 3\n"
  "s_and_b32 s43, s39, 1\n"
  "s_lshl_b32 s54, s44, 2\n"
  "s_add_u32 s54, s54, s43\n"
  "s_lshl_b32 s54, s54, 16\n"
  "s_lshl_b32 s60, s44, 4\n"
  "s_add_u32 s60, s60, s43\n"
  "s_lshl_b32 s60, s60, 12\n"
  "s_add_u32 s60, s60, s94\n"
  "s_add_u32 s39, s39, 1\n"
  "s_cmp_ge_u32 s39, 6\n"
  "s_cselect_b32 s44, 6, 0\n"
  "s_sub_u32 s39, s39, s44\n"
  "s_lshr_b32 s44, s39, 1\n"
  "s_add_u32 s44, s44, s34\n"
  "s_add_u32 s44, s44, 1\n"
  "s_and_b32 s44, s44, 3\n"
  "s_and_b32 s43, s39, 1\n"
  "s_lshl_b32 s55, s44, 2\n"
  "s_add_u32 s55, s55, s43\n"
  "s_lshl_b32 s55, s55, 16\n"
  "s_lshl_b32 s61, s44, 4\n"
  "s_add_u32 s61, s61, s43\n"
  "s_lshl_b32 s61, s61, 12\n"
  "s_add_u32 s61, s61, s94\n"
  "s_add_u32 s39, s39, 1\n"
  "s_cmp_ge_u32 s39, 6\n"
  "s_cselect_b32 s44, 6, 0\n"
  "s_sub_u32 s39, s39, s44\n"
  "s_lshr_b32 s44, s39, 1\n"
  "s_add_u32 s44, s44, s34\n"
  "s_add_u32 s44, s44, 1\n"
  "s_and_b32 s44, s44, 3\n"
  "s_and_b32 s43, s39, 1\n"
  "s_lshl_b32 s56, s44, 2\n"
  "s_add_u32 s56, s56, s43\n"
  "s_lshl_b32 s56, s56, 16\n"
  "s_lshl_b32 s62, s44, 4\n"
  "s_add_u32 s62, s62, s43\n"
  "s_lshl_b32 s62, s62, 12\n"
  "s_add_u32 s62, s62, s94\n"
  "s_add_u32 s39, s39, 1\n"
  "s_cmp_ge_u32 s39, 6\n"
  "s_cselect_b32 s44, 6, 0\n"
  "s_sub_u32 s39, s39, s44\n"
  "s_lshr_b32 s44, s39, 1\n"
  "s_add_u32 s44, s44, s34\n"
  "s_add_u32 s44, s44, 1\n"
  "s_and_b32 s44, s44, 3\n"
  "s_and_b32 s43, s39, 1\n"
  "s_lshl_b32 s57, s44, 2\n"
  "s_add_u32 s57, s57, s43\n"
  "s_lshl_b32 s57, s57, 16\n"
  "s_lshl_b32 s63, s44, 4\n"
  "s_add_u32 s63, s63, s43\n"
  "s_lshl_b32 s63, s63, 12\n"
  "s_add_u32 s63, s63, s94\n"
  "s_add_u32 s39, s39, 1\n"
  "s_cmp_ge_u32 s39, 6\n"
  "s_cselect_b32 s44, 6, 0\n"
  "s_sub_u32 s39, s39, s44\n"
  "s_lshr_b32 s44, s39, 1\n"
  "s_add_u32 s44, s44, s34\n"
  "s_add_u32 s44, s44, 1\n"
  "s_and_b32 s44, s44, 3\n"
  "s_and_b32 s43, s39, 1\n"
  "s_lshl_b32 s58, s44, 2\n"
  "s_add_u32 s58, s58, s43\n"
  "s_lshl_b32 s58, s58, 16\n"
  "s_lshl_b32 s64, s44, 4\n"
  "s_add_u32 s64, s64, s43\n"
  "s_lshl_b32 s64, s64, 12\n"
  "s_add_u32 s64, s64, s94\n"
  "s_add_u32 s39, s39, 1\n"
  "s_cmp_ge_u32 s39, 6\n"
  "s_cselect_b32 s44, 6, 0\n"
  "s_sub_u32 s39, s39, s44\n"
  "s_lshr_b32 s44, s39, 1\n"
  "s_add_u32 s44, s44, s34\n"
  "s_add_u32 s44, s44, 1\n"
  "s_and_b32 s44, s44, 3\n"
  "s_and_b32 s43, s39, 1\n"
  "s_lshl_b32 s59, s44, 2\n"
  "s_add_u32 s59, s59, s43\n"
  "s_lshl_b32 s59, s59, 16\n"
  "s_lshl_b32 s65, s44, 4\n"
  "s_add_u32 s65, s65, s43\n"
  "s_lshl_b32 s65, s65, 12\n"
  "s_add_u32 s65, s65, s94\n"
  "s_add_u32 s39, s39, 1\n"
  "s_bfe_u32 s39, %[wg], 0x10005\n"
  "s_cmp_eq_u32 s39, 1\n"
  "s_cbranch_scc1 Lcls1\n"
  "s_waitcnt vmcnt(4)\n"
  "s_barrier\n"
  "s_lshl_b32 s91, s69, 7\n"
  "s_add_u32 m0, s37, 81920\n"
  "s_nop 0\n"
  "buffer_load_dwordx4 v2, s[4:7], s91 offen lds\n"
  "buffer_load_dwordx4 v3, s[4:7], s91 offen offset:1024 lds\n"
  "buffer_load_dwordx4 v4, s[4:7], s91 offen offset:2048 lds\n"
  "buffer_load_dwordx4 v5, s[4:7], s91 offen offset:3072 lds\n"
  "s_lshl_b32 s91, s69, 19\n"
  "s_add_u32 m0, s37, 32768\n"
  "s_nop 0\n"
  "buffer_load_dwordx4 v6, s[8:11], s91 offen lds\n"
  "buffer_load_dwordx4 v7, s[8:11], s91 offen offset:1024 lds\n"
  "buffer_load_dwordx4 v8, s[8:11], s91 offen offset:2048 lds\n"
  "buffer_load_dwordx4 v9, s[8:11], s91 offen offset:3072 lds\n"
  "s_waitcnt vmcnt(8)\n"
  "s_barrier\n"
  "s_lshl_b32 s91, s69, 19\n"
  "s_add_u32 s91, s91, 256\n"
  "s_add_u32 m0, s37, 49152\n"
  "s_nop 0\n"
  "buffer_load_dwordx4 v6, s[8:11], s91 offen lds\n"
  "buffer_load_dwordx4 v7, s[8:11], s91 offen offset:1024 lds\n"
  "buffer_load_dwordx4 v8, s[8:11], s91 offen offset:2048 lds\n"
  "buffer_load_dwordx4 v9, s[8:11], s91 offen offset:3072 lds\n"
  "s_lshl_b32 s91, s70, 7\n"
  "s_add_u32 m0, s37, 65536\n"
  "s_nop 0\n"
  "buffer_load_dwordx4 v2, s[4:7], s91 offen lds\n"
  "buffer_load_dwordx4 v3, s[4:7], s91 offen offset:1024 lds\n"
  "buffer_load_dwordx4 v4, s[4:7], s91 offen offset:2048 lds\n"
  "buffer_load_dwordx4 v5, s[4:7], s91 offen offset:3072 lds\n"
  "s_waitcnt vmcnt(8)\n"
  "s_barrier\n"
  "s_lshl_b32 s91, s70, 19\n"
  "s_add_u32 m0, s37, 0\n"
  "s_nop 0\n"
  "buffer_load_dwordx4 v6, s[8:11], s91 offen lds\n"
  "buffer_load_dwordx4 v7, s[8:11], s91 offen offset:1024 lds\n"
  "buffer_load_dwordx4 v8, s[8:11], s91 offen offset:2048 lds\n"
  "buffer_load_dwordx4 v9, s[8:11], s91 offen offset:3072 lds\n"
  "s_mov_b32 s91, 0\n"
  "s_add_u32 m0, s37, 98304\n"
  "s_nop 0\n"
  "buffer_load_dwordx4 v18, s[12:15], s91 offen nt lds\n"
  "buffer_load_dwordx4 v19, s[12:15], s91 offen offset:1024 nt lds\n"
  "buffer_load_dwordx4 v20, s[12:15], s91 offen offset:2048 nt lds\n"
  "buffer_load_dwordx4 v21, s[12:15], s91 offen offset:3072 nt lds\n"
  "s_mov_b32 s91, 524288\n"
  "s_add_u32 m0, s37, 114688\n"
  "s_nop 0\n"
  "buffer_load_dwordx4 v18, s[12:15], s91 offen nt lds\n"
  "buffer_load_dwordx4 v19, s[12:15], s91 offen offset:1024 nt lds\n"
  "buffer_load_dwordx4 v20, s[12:15], s91 offen offset:2048 nt lds\n"
  "buffer_load_dwordx4 v21, s[12:15], s91 offen offset:3072 nt lds\n"
  "s_waitcnt vmcnt(16)\n"
  "s_barrier\n"
  "s_lshl_b32 s91, s70, 19\n"
  "s_add_u32 s91, s91, 256\n"
  "s_add_u32 m0, s37, 16384\n"
  "s_nop 0\n"
  "buffer_load_dwordx4 v6, s[8:11], s91 offen lds\n"
  "buffer_load_dwordx4 v7, s[8:11], s91 offen offset:1024 lds\n"
  "buffer_load_dwordx4 v8, s[8:11], s91 offen offset:2048 lds\n"
  "buffer_load_dwordx4 v9, s[8:11], s91 offen offset:3072 lds\n"
  "s_lshl_b32 s91, s71, 7\n"
  "s_add_u32 m0, s37, 81920\n"
  "s_nop 0\n"
  "buffer_load_dwordx4 v2, s[4:7], s91 offen lds\n"
  "buffer_load_dwordx4 v3, s[4:7], s91 offen offset:1024 lds\n"
  "buffer_load_dwordx4 v4, s[4:7], s91 offen offset:2048 lds\n"
  "buffer_load_dwordx4 v5, s[4:7], s91 offen offset:3072 lds\n"
  "s_waitcnt vmcnt(16)\n"
  "s_barrier\n"
  "s_lshl_b32 s91, s71, 19\n"
  "s_add_u32 m0, s37, 32768\n"
  "s_nop 0\n"
  "buffer_load_dwordx4 v6, s[8:11], s91 offen lds\n"
  "buffer_load_dwordx4 v7, s[8:11], s91 offen offset:1024 lds\n"
  "buffer_load_dwordx4 v8, s[8:11], s91 offen offset:2048 lds\n"
  "buffer_load_dwordx4 v9, s[8:11], s91 offen offset:3072 lds\n"
  "s_waitcnt vmcnt(8)\n"
  "s_barrier\n"
  "s_lshl_b32 s91, s71, 19\n"
  "s_add_u32 s91, s91, 256\n"
  "s_add_u32 m0, s37, 49152\n"
  "s_nop 0\n"
  "buffer_load_dwordx4 v6, s[8:11], s91 offen lds\n"
  "buffer_load_dwordx4 v7, s[8:11], s91 offen offset:1024 lds\n"
  "buffer_load_dwordx4 v8, s[8:11], s91 offen offset:2048 lds\n"
  "buffer_load_dwordx4 v9, s[8:11], s91 offen offset:3072 lds\n"
  "s_lshl_b32 s91, s72, 7\n"
  "s_add_u32 m0, s37, 65536\n"
  "s_nop 0\n"
  "buffer_load_dwordx4 v2, s[4:7], s91 offen lds\n"
  "buffer_load_dwordx4 v3, s[4:7], s91 offen offset:1024 lds\n"
  "buffer_load_dwordx4 v4, s[4:7], s91 offen offset:2048 lds\n"
  "buffer_load_dwordx4 v5, s[4:7], s91 offen offset:3072 lds\n"
  "s_waitcnt vmcnt(8)\n"
  "s_barrier\n"
  "s_lshl_b32 s91, s72, 19\n"
  "s_add_u32 m0, s37, 0\n"
  "s_nop 0\n"
  "buffer_load_dwordx4 v6, s[8:11], s91 offen lds\n"
  "buffer_load_dwordx4 v7, s[8:11], s91 offen offset:1024 lds\n"
  "buffer_load_dwordx4 v8, s[8:11], s91 offen offset:2048 lds\n"
  "buffer_load_dwordx4 v9, s[8:11], s91 offen offset:3072 lds\n"
  "s_mov_b32 s91, 0\n"
  "s_add_u32 m0, s37, 131072\n"
  "s_nop 0\n"
  "buffer_load_dwordx4 v14, s[16:19], s91 offen nt lds\n"
  "buffer_load_dwordx4 v15, s[16:19], s91 offen offset:1024 nt lds\n"
  "buffer_load_dwordx4 v16, s[16:19], s91 offen offset:2048 nt lds\n"
  "buffer_load_dwordx4 v17, s[16:19], s91 offen offset:3072 nt lds\n"
  "s_mov_b32 s91, 524288\n"
  "s_add_u32 m0, s37, 147456\n"
  "s_nop 0\n"
  "buffer_load_dwordx4 v14, s[16:19], s91 offen nt lds\n"
  "buffer_load_dwordx4 v15, s[16:19], s91 offen offset:1024 nt lds\n"
  "buffer_load_dwordx4 v16, s[16:19], s91 offen offset:2048 nt lds\n"
  "buffer_load_dwordx4 v17, s[16:19], s91 offen offset:3072 nt lds\n"
  "s_waitcnt vmcnt(16)\n"
  "s_barrier\n"
  "s_lshl_b32 s91, s72, 19\n"
  "s_add_u32 s91, s91, 256\n"
  "s_add_u32 m0, s37, 16384\n"
  "s_nop 0\n"
  "buffer_load_dwordx4 v6, s[8:11], s91 offen lds\n"
  "buffer_load_dwordx4 v7, s[8:11], s91 offen offset:1024 lds\n"
  "buffer_load_dwordx4 v8, s[8:11], s91 offen offset:2048 lds\n"
  "buffer_load_dwordx4 v9, s[8:11], s91 offen offset:3072 lds\n"
  "s_lshl_b32 s91, s73, 7\n"
  "s_add_u32 m0, s37, 81920\n"
  "s_nop 0\n"
  "buffer_load_dwordx4 v2, s[4:7], s91 offen lds\n"
  "buffer_load_dwordx4 v3, s[4:7], s91 offen offset:1024 lds\n"
  "buffer_load_dwordx4 v4, s[4:7], s91 offen offset:2048 lds\n"
  "buffer_load_dwordx4 v5, s[4:7], s91 offen offset:3072 lds\n"
  "s_waitcnt vmcnt(16)\n"
  "s_barrier\n"
  "s_lshl_b32 s91, s73, 19\n"
  "s_add_u32 m0, s37, 32768\n"
  "s_nop 0\n"
  "buffer_load_dwordx4 v6, s[8:11], s91 offen lds\n"
  "buffer_load_dwordx4 v7, s[8:11], s91 offen offset:1024 lds\n"
  "buffer_load_dwordx4 v8, s[8:11], s91 offen offset:2048 lds\n"
  "buffer_load_dwordx4 v9, s[8:11], s91 offen offset:3072 lds\n"
  "s_waitcnt lgkmcnt(0)\n"
  "s_add_u32 s46, s50, 0x9e3779b9\n"
  "s_add_u32 s47, s51, 0x7f4a7c15\n"
  "s_add_u32 s50, s50, 1\n"
  "v_mov_b32 v26, s50\n"
  "v_mov_b32 v27, s51\n"
  "s_waitcnt vmcnt(8)\n"
  "s_barrier\n"
  "s_lshl_b32 s91, s73, 19\n"
  "s_add_u32 s91, s91, 256\n"
  "s_add_u32 m0, s37, 49152\n"
  "s_nop 0\n"
  "buffer_load_dwordx4 v6, s[8:11], s91 offen lds\n"
  "buffer_load_dwordx4 v7, s[8:11], s91 offen offset:1024 lds\n"
  "buffer_load_dwordx4 v8, s[8:11], s91 offen offset:2048 lds\n"
  "buffer_load_dwordx4 v9, s[8:11], s91 offen offset:3072 lds\n"
  "s_lshl_b32 s91, s74, 7\n"
  "s_add_u32 m0, s37, 65536\n"
  "s_nop 0\n"
  "buffer_load_dwordx4 v2, s[4:7], s91 offen lds\n"
  "buffer_load_dwordx4 v3, s[4:7], s91 offen offset:1024 lds\n"
  "buffer_load_dwordx4 v4, s[4:7], s91 offen offset:2048 lds\n"
  "buffer_load_dwordx4 v5, s[4:7], s91 offen offset:3072 lds\n"
  "s_waitcnt vmcnt(8)\n"
  "s_barrier\n"
  "s_lshl_b32 s91, s74, 19\n"
  "s_add_u32 m0, s37, 0\n"
  "s_nop 0\n"
  "buffer_load_dwordx4 v6, s[8:11], s91 offen lds\n"
  "buffer_load_dwordx4 v7, s[8:11], s91 offen offset:1024 lds\n"
  "buffer_load_dwordx4 v8, s[8:11], s91 offen offset:2048 lds\n"
  "buffer_load_dwordx4 v9, s[8:11], s91 offen offset:3072 lds\n"
  "s_mov_b32 s91, 16384\n"
  "buffer_load_dwordx4 v[104:107], v18, s[12:15], s91 offen nt\n"
  "buffer_load_dwordx4 v[108:111], v19, s[12:15], s91 offen offset:1024 nt\n"
  "buffer_load_dwordx4 v[112:115], v20, s[12:15], s91 offen offset:2048 nt\n"
  "buffer_load_dwordx4 v[116:119], v21, s[12:15], s91 offen offset:3072 nt\n"
  "s_mov_b32 s91, 540672\n"
  "buffer_load_dwordx4 v[120:123], v18, s[12:15], s91 offen nt\n"
  "buffer_load_dwordx4 v[124:127], v19, s[12:15], s91 offen offset:1024 nt\n"
  "buffer_load_dwordx4 v[128:131], v20, s[12:15], s91 offen offset:2048 nt\n"
  "buffer_load_dwordx4 v[132:135], v21, s[12:15], s91 offen offset:3072 nt\n"
  "s_waitcnt vmcnt(16)\n"
  "s_barrier\n"
  "s_lshl_b32 s91, s75, 19\n"
  "s_add_u32 m0, s37, 16384\n"
  "s_nop 0\n"
  "buffer_load_dwordx4 v6, s[8:11], s91 offen lds\n"
  "buffer_load_dwordx4 v7, s[8:11], s91 offen offset:1024 lds\n"
  "buffer_load_dwordx4 v8, s[8:11], s91 offen offset:2048 lds\n"
  "buffer_load_dwordx4 v9, s[8:11], s91 offen offset:3072 lds\n"
  "s_lshl_b32 s91, s75, 7\n"
  "s_add_u32 m0, s37, 81920\n"
  "s_nop 0\n"
  "buffer_load_dwordx4 v2, s[4:7], s91 offen lds\n"
  "buffer_load_dwordx4 v3, s[4:7], s91 offen offset:1024 lds\n"
  "buffer_load_dwordx4 v4, s[4:7], s91 offen offset:2048 lds\n"
  "buffer_load_dwordx4 v5, s[4:7], s91 offen offset:3072 lds\n"
  "s_waitcnt vmcnt(16)\n"
  "s_barrier\n"
  "s_lshl_b32 s91, s74, 19\n"
  "s_add_u32 s91, s91, 256\n"
  "s_add_u32 m0, s37, 32768\n"
  "s_nop 0\n"
  "buffer_load_dwordx4 v6, s[8:11], s91 offen lds\n"
  "buffer_load_dwordx4 v7, s[8:11], s91 offen offset:1024 lds\n"
  "buffer_load_dwordx4 v8, s[8:11], s91 offen offset:2048 lds\n"
  "buffer_load_dwordx4 v9, s[8:11], s91 offen offset:3072 lds\n"
  "s_waitcnt vmcnt(4)\n"
  "s_barrier\n"
  "s_lshl_b32 s91, s75, 19\n"
  "s_add_u32 s91, s91, 256\n"
  "s_add_u32 m0, s37, 49152\n"
  "s_nop 0\n"
  "buffer_load_dwordx4 v6, s[8:11], s91 offen lds\n"
  "buffer_load_dwordx4 v7, s[8:11], s91 offen offset:1024 lds\n"
  "buffer_load_dwordx4 v8, s[8:11], s91 offen offset:2048 lds\n"
  "buffer_load_dwordx4 v9, s[8:11], s91 offen offset:3072 lds\n"
  "s_barrier\n"
  "s_mov_b32 s91, 16384\n"
  "buffer_load_dwordx4 v[136:139], v14, s[16:19], s91 offen nt\n"
  "buffer_load_dwordx4 v[140:143], v15, s[16:19], s91 offen offset:1024 nt\n"
  "buffer_load_dwordx4 v[144:147], v16, s[16:19], s91 offen offset:2048 nt\n"
  "buffer_load_dwordx4 v[148:151], v17, s[16:19], s91 offen offset:3072 nt\n"
  "s_mov_b32 s91, 540672\n"
  "buffer_load_dwordx4 v[152:155], v14, s[16:19], s91 offen nt\n"
  "buffer_load_dwordx4 v[156:159], v15, s[16:19], s91 offen offset:1024 nt\n"
  "buffer_load_dwordx4 v[160:163], v16, s[16:19], s91 offen offset:2048 nt\n"
  "buffer_load_dwordx4 v[164:167], v17, s[16:19], s91 offen offset:3072 nt\n"
  "s_barrier\n"
  "s_barrier\n"
  "ds_write_b128 v36, v[104:107] offset:0\n"
  "ds_write_b128 v36, v[108:111] offset:1024\n"
  "ds_write_b128 v36, v[112:115] offset:2048\n"
  "ds_write_b128 v36, v[116:119] offset:3072\n"
  "ds_write_b128 v36, v[120:123] offset:16384\n"
  "ds_write_b128 v36, v[124:127] offset:17408\n"
  "ds_write_b128 v36, v[128:131] offset:18432\n"
  "ds_write_b128 v36, v[132:135] offset:19456\n"
  "s_waitcnt lgkmcnt(0)\n"
  "s_barrier\n"
  "s_waitcnt vmcnt(12)\n"
  "s_barrier\n"
  "s_waitcnt vmcnt(8)\n"
  "s_barrier\n"
  "s_add_u32 s91, s66, 0\n"
  "s_add_u32 m0, s37, 131072\n"
  "s_nop 0\n"
  "buffer_load_dwordx4 v10, s[20:23], s91 offen lds\n"
  "buffer_load_dwordx4 v11, s[20:23], s91 offen offset:1024 lds\n"
  "buffer_load_dwordx4 v12, s[20:23], s91 offen offset:2048 lds\n"
  "buffer_load_dwordx4 v13, s[20:23], s91 offen offset:3072 lds\n"
  "s_add_u32 s91, s66, 65536\n"
  "s_add_u32 m0, s37, 147456\n"
  "s_nop 0\n"
  "buffer_load_dwordx4 v10, s[20:23], s91 offen lds\n"
  "buffer_load_dwordx4 v11, s[20:23], s91 offen offset:1024 lds\n"
  "buffer_load_dwordx4 v12, s[20:23], s91 offen offset:2048 lds\n"
  "buffer_load_dwordx4 v13, s[20:23], s91 offen offset:3072 lds\n"
  "s_barrier\n"
  "v_add_u32 v36, 65536, v36\n"
  "s_waitcnt vmcnt(12)\n"
  "ds_write_b128 v36, v[136:139] offset:32768\n"
  "ds_write_b128 v36, v[140:143] offset:33792\n"
  "ds_write_b128 v36, v[144:147] offset:34816\n"
  "ds_write_b128 v36, v[148:151] offset:35840\n"
  "s_add_u32 s91, s66, 131072\n"
  "s_add_u32 m0, s37, 32768\n"
  "s_nop 0\n"
  "buffer_load_dwordx4 v10, s[20:23], s91 offen lds\n"
  "buffer_load_dwordx4 v11, s[20:23], s91 offen offset:1024 lds\n"
  "buffer_load_dwordx4 v12, s[20:23], s91 offen offset:2048 lds\n"
  "buffer_load_dwordx4 v13, s[20:23], s91 offen offset:3072 lds\n"
  "s_waitcnt lgkmcnt(0)\n"
  "s_barrier\n"
  "s_waitcnt vmcnt(12)\n"
  "ds_write_b128 v36, v[152:155] offset:49152\n"
  "ds_write_b128 v36, v[156:159] offset:50176\n"
  "ds_write_b128 v36, v[160:163] offset:51200\n"
  "ds_write_b128 v36, v[164:167] offset:52224\n"
  "s_add_u32 s91, s66, 196608\n"
  "s_add_u32 m0, s37, 49152\n"
  "s_nop 0\n"
  "buffer_load_dwordx4 v10, s[20:23], s91 offen lds\n"
  "buffer_load_dwordx4 v11, s[20:23], s91 offen offset:1024 lds\n"
  "buffer_load_dwordx4 v12, s[20:23], s91 offen offset:2048 lds\n"
  "buffer_load_dwordx4 v13, s[20:23], s91 offen offset:3072 lds\n"
  "s_waitcnt lgkmcnt(0)\n"
  "s_barrier\n"
  "s_add_u32 s91, s54, 0\n"
  "s_add_u32 m0, s37, 0\n"
  "s_nop 0\n"
  "buffer_load_dwordx4 v10, s[20:23], s91 offen lds\n"
  "buffer_load_dwordx4 v11, s[20:23], s91 offen offset:1024 lds\n"
  "buffer_load_dwordx4 v12, s[20:23], s91 offen offset:2048 lds\n"
  "buffer_load_dwordx4 v13, s[20:23], s91 offen offset:3072 lds\n"
  "s_add_u32 s91, s55, 0\n"
  "s_add_u32 m0, s37, 16384\n"
  "s_nop 0\n"
  "buffer_load_dwordx4 v10, s[20:23], s91 offen lds\n"
  "buffer_load_dwordx4 v11, s[20:23], s91 offen offset:1024 lds\n"
  "buffer_load_dwordx4 v12, s[20:23], s91 offen offset:2048 lds\n"
  "buffer_load_dwordx4 v13, s[20:23], s91 offen offset:3072 lds\n"
  "s_barrier\n"
  "buffer_load_dwordx2 v[24:25], v23, s[24:27], s68 offen sc1\n"
  "s_waitcnt vmcnt(0)\n"
  "v_cmp_ne_u64 vcc, s[46:47], v[24:25]\n"
  "s_cbranch_vccnz Lslow_o0c0\n"
  "Lback_o0c0:\n"
  "s_barrier\n"
  "s_barrier\n"
  "s_add_u32 s91, s60, 0\n"
  "s_add_u32 m0, s37, 65536\n"
  "s_nop 0\n"
  "buffer_load_dwordx4 v22, s[24:27], s91 offen sc1 lds\n"
  "buffer_load_dwordx4 v22, s[24:27], s91 offen offset:1024 sc1 lds\n"
  "buffer_load_dwordx4 v22, s[24:27], s91 offen offset:2048 sc1 lds\n"
  "buffer_load_dwordx4 v22, s[24:27], s91 offen offset:3072 sc1 lds\n"
  "s_add_u32 s91, s61, 0\n"
  "s_add_u32 m0, s37, 81920\n"
  "s_nop 0\n"
  "buffer_load_dwordx4 v22, s[24:27], s91 offen sc1 lds\n"
  "buffer_load_dwordx4 v22, s[24:27], s91 offen offset:1024 sc1 lds\n"
  "buffer_load_dwordx4 v22, s[24:27], s91 offen offset:2048 sc1 lds\n"
  "buffer_load_dwordx4 v22, s[24:27], s91 offen offset:3072 sc1 lds\n"
  "s_barrier\n"
  "s_add_u32 s91, s56, 0\n"
  "s_add_u32 m0, s37, 131072\n"
  "s_nop 0\n"
  "buffer_load_dwordx4 v10, s[20:23], s91 offen lds\n"
  "buffer_load_dwordx4 v11, s[20:23], s91 offen offset:1024 lds\n"
  "buffer_load_dwordx4 v12, s[20:23], s91 offen offset:2048 lds\n"
  "buffer_load_dwordx4 v13, s[20:23], s91 offen offset:3072 lds\n"
  "s_mov_b64 exec, 1\n"
  "buffer_store_dwordx2 v[26:27], v28, s[24:27], s78 offen sc1\n"
  "s_mov_b64 exec, -1\n"
  "s_barrier\n"
  "s_add_u32 s91, s62, 0\n"
  "s_add_u32 m0, s37, 98304\n"
  "s_nop 0\n"
  "buffer_load_dwordx4 v22, s[24:27], s91 offen sc1 lds\n"
  "buffer_load_dwordx4 v22, s[24:27], s91 offen offset:1024 sc1 lds\n"
  "buffer_load_dwordx4 v22, s[24:27], s91 offen offset:2048 sc1 lds\n"
  "buffer_load_dwordx4 v22, s[24:27], s91 offen offset:3072 sc1 lds\n"
  "s_add_u32 s91, s57, 0\n"
  "s_add_u32 m0, s37, 147456\n"
  "s_nop 0\n"
  "buffer_load_dwordx4 v10, s[20:23], s91 offen lds\n"
  "buffer_load_dwordx4 v11, s[20:23], s91 offen offset:1024 lds\n"
  "buffer_load_dwordx4 v12, s[20:23], s91 offen offset:2048 lds\n"
  "buffer_load_dwordx4 v13, s[20:23], s91 offen offset:3072 lds\n"
  "s_waitcnt vmcnt(17)\n"
  "s_barrier\n"
  "s_add_u32 s91, s63, 0\n"
  "s_add_u32 m0, s37, 114688\n"
  "s_nop 0\n"
  "buffer_load_dwordx4 v22, s[24:27], s91 offen sc1 lds\n"
  "buffer_load_dwordx4 v22, s[24:27], s91 offen offset:1024 sc1 lds\n"
  "buffer_load_dwordx4 v22, s[24:27], s91 offen offset:2048 sc1 lds\n"
  "buffer_load_dwordx4 v22, s[24:27], s91 offen offset:3072 sc1 lds\n"
  "s_add_u32 s91, s58, 0\n"
  "s_add_u32 m0, s37, 32768\n"
  "s_nop 0\n"
  "buffer_load_dwordx4 v10, s[20:23], s91 offen lds\n"
  "buffer_load_dwordx4 v11, s[20:23], s91 offen offset:1024 lds\n"
  "buffer_load_dwordx4 v12, s[20:23], s91 offen offset:2048 lds\n"
  "buffer_load_dwordx4 v13, s[20:23], s91 offen offset:3072 lds\n"
  "s_waitcnt vmcnt(21)\n"
  "s_barrier\n"
  "s_add_u32 s91, s64, 0\n"
  "s_add_u32 m0, s37, 65536\n"
  "s_nop 0\n"
  "buffer_load_dwordx4 v22, s[24:27], s91 offen sc1 lds\n"
  "buffer_load_dwordx4 v22, s[24:27], s91 offen offset:1024 sc1 lds\n"
  "buffer_load_dwordx4 v22, s[24:27], s91 offen offset:2048 sc1 lds\n"
  "buffer_load_dwordx4 v22, s[24:27], s91 offen offset:3072 sc1 lds\n"
  "s_add_u32 s91, s59, 0\n"
  "s_add_u32 m0, s37, 49152\n"
  "s_nop 0\n"
  "buffer_load_dwordx4 v10, s[20:23], s91 offen lds\n"
  "buffer_load_dwordx4 v11, s[20:23], s91 offen offset:1024 lds\n"
  "buffer_load_dwordx4 v12, s[20:23], s91 offen offset:2048 lds\n"
  "buffer_load_dwordx4 v13, s[20:23], s91 offen offset:3072 lds\n"
  "s_waitcnt vmcnt(20)\n"
  "s_barrier\n"
  "buffer_load_dwordx2 v[24:25], v23, s[24:27], s68 offen offset:8 sc1\n"
  "s_add_u32 s91, s65, 0\n"
  "s_add_u32 m0, s37, 81920\n"
  "s_nop 0\n"
  "buffer_load_dwordx4 v22, s[24:27], s91 offen sc1 lds\n"
  "buffer_load_dwordx4 v22, s[24:27], s91 offen offset:1024 sc1 lds\n"
  "buffer_load_dwordx4 v22, s[24:27], s91 offen offset:2048 sc1 lds\n"
  "buffer_load_dwordx4 v22, s[24:27], s91 offen offset:3072 sc1 lds\n"
  "s_add_u32 s91, s54, 131072\n"
  "s_add_u32 m0, s37, 0\n"
  "s_nop 0\n"
  "buffer_load_dwordx4 v10, s[20:23], s91 offen lds\n"
  "buffer_load_dwordx4 v11, s[20:23], s91 offen offset:1024 lds\n"
  "buffer_load_dwordx4 v12, s[20:23], s91 offen offset:2048 lds\n"
  "buffer_load_dwordx4 v13, s[20:23], s91 offen offset:3072 lds\n"
  "s_waitcnt vmcnt(8)\n"
  "v_cmp_ne_u64 vcc, s[46:47], v[24:25]\n"
  "s_cbranch_vccnz Lslow_o1c0\n"
  "Lback_o1c0:\n"
  "s_barrier\n"
  "s_add_u32 s91, s60, 32768\n"
  "s_add_u32 m0, s37, 98304\n"
  "s_nop 0\n"
  "buffer_load_dwordx4 v22, s[24:27], s91 offen sc1 lds\n"
  "buffer_load_dwordx4 v22, s[24:27], s91 offen offset:1024 sc1 lds\n"
  "buffer_load_dwordx4 v22, s[24:27], s91 offen offset:2048 sc1 lds\n"
  "buffer_load_dwordx4 v22, s[24:27], s91 offen offset:3072 sc1 lds\n"
  "s_add_u32 s91, s55, 131072\n"
  "s_add_u32 m0, s37, 16384\n"
  "s_nop 0\n"
  "buffer_load_dwordx4 v10, s[20:23], s91 offen lds\n"
  "buffer_load_dwordx4 v11, s[20:23], s91 offen offset:1024 lds\n"
  "buffer_load_dwordx4 v12, s[20:23], s91 offen offset:2048 lds\n"
  "buffer_load_dwordx4 v13, s[20:23], s91 offen offset:3072 lds\n"
  "s_barrier\n"
  "s_add_u32 s91, s61, 32768\n"
  "s_add_u32 m0, s37, 114688\n"
  "s_nop 0\n"
  "buffer_load_dwordx4 v22, s[24:27], s91 offen sc1 lds\n"
  "buffer_load_dwordx4 v22, s[24:27], s91 offen offset:1024 sc1 lds\n"
  "buffer_load_dwordx4 v22, s[24:27], s91 offen offset:2048 sc1 lds\n"
  "buffer_load_dwordx4 v22, s[24:27], s91 offen offset:3072 sc1 lds\n"
  "s_add_u32 s91, s56, 131072\n"
  "s_add_u32 m0, s37, 131072\n"
  "s_nop 0\n"
  "buffer_load_dwordx4 v10, s[20:23], s91 offen lds\n"
  "buffer_load_dwordx4 v11, s[20:23], s91 offen offset:1024 lds\n"
  "buffer_load_dwordx4 v12, s[20:23], s91 offen offset:2048 lds\n"
  "buffer_load_dwordx4 v13, s[20:23], s91 offen offset:3072 lds\n"
  "s_waitcnt vmcnt(20)\n"
  "s_barrier\n"
  "s_add_u32 s91, s62, 32768\n"
  "s_add_u32 m0, s37, 65536\n"
  "s_nop 0\n"
  "buffer_load_dwordx4 v22, s[24:27], s91 offen sc1 lds\n"
  "buffer_load_dwordx4 v22, s[24:27], s91 offen offset:1024 sc1 lds\n"
  "buffer_load_dwordx4 v22, s[24:27], s91 offen offset:2048 sc1 lds\n"
  "buffer_load_dwordx4 v22, s[24:27], s91 offen offset:3072 sc1 lds\n"
  "s_add_u32 s91, s57, 131072\n"
  "s_add_u32 m0, s37, 147456\n"
  "s_nop 0\n"
  "buffer_load_dwordx4 v10, s[20:23], s91 offen lds\n"
  "buffer_load_dwordx4 v11, s[20:23], s91 offen offset:1024 lds\n"
  "buffer_load_dwordx4 v12, s[20:23], s91 offen offset:2048 lds\n"
  "buffer_load_dwordx4 v13, s[20:23], s91 offen offset:3072 lds\n"
  "s_waitcnt vmcnt(20)\n"
  "s_barrier\n"
  "s_add_u32 s91, s63, 32768\n"
  "s_add_u32 m0, s37, 81920\n"
  "s_nop 0\n"
  "buffer_load_dwordx4 v22, s[24:27], s91 offen sc1 lds\n"
  "buffer_load_dwordx4 v22, s[24:27], s91 offen offset:1024 sc1 lds\n"
  "buffer_load_dwordx4 v22, s[24:27], s91 offen offset:2048 sc1 lds\n"
  "buffer_load_dwordx4 v22, s[24:27], s91 offen offset:3072 sc1 lds\n"
  "s_add_u32 s91, s58, 131072\n"
  "s_add_u32 m0, s37, 32768\n"
  "s_nop 0\n"
  "buffer_load_dwordx4 v10, s[20:23], s91 offen lds\n"
  "buffer_load_dwordx4 v11, s[20:23], s91 offen offset:1024 lds\n"
  "buffer_load_dwordx4 v12, s[20:23], s91 offen offset:2048 lds\n"
  "buffer_load_dwordx4 v13, s[20:23], s91 offen offset:3072 lds\n"
  "s_waitcnt vmcnt(20)\n"
  "s_barrier\n"
  "s_add_u32 s91, s64, 32768\n"
  "s_add_u32 m0, s37, 98304\n"
  "s_nop 0\n"
  "buffer_load_dwordx4 v22, s[24:27], s91 offen sc1 lds\n"
  "buffer_load_dwordx4 v22, s[24:27], s91 offen offset:1024 sc1 lds\n"
  "buffer_load_dwordx4 v22, s[24:27], s91 offen offset:2048 sc1 lds\n"
  "buffer_load_dwordx4 v22, s[24:27], s91 offen offset:3072 sc1 lds\n"
  "s_add_u32 s91, s59, 131072\n"
  "s_add_u32 m0, s37, 49152\n"
  "s_nop 0\n"
  "buffer_load_dwordx4 v10, s[20:23], s91 offen lds\n"
  "buffer_load_dwordx4 v11, s[20:23], s91 offen offset:1024 lds\n"
  "buffer_load_dwordx4 v12, s[20:23], s91 offen offset:2048 lds\n"
  "buffer_load_dwordx4 v13, s[20:23], s91 offen offset:3072 lds\n"
  "s_waitcnt vmcnt(20)\n"
  "s_barrier\n"
  "s_add_u32 s91, s65, 32768\n"
  "s_add_u32 m0, s37, 114688\n"
  "s_nop 0\n"
  "buffer_load_dwordx4 v22, s[24:27], s91 offen sc1 lds\n"
  "buffer_load_dwordx4 v22, s[24:27], s91 offen offset:1024 sc1 lds\n"
  "buffer_load_dwordx4 v22, s[24:27], s91 offen offset:2048 sc1 lds\n"
  "buffer_load_dwordx4 v22, s[24:27], s91 offen offset:3072 sc1 lds\n"
  "s_waitcnt vmcnt(16)\n"
  "s_barrier\n"
  "s_waitcnt vmcnt(8)\n"
  "s_barrier\n"
  "s_waitcnt vmcnt(0)\n"
  "s_barrier\n"
  "s_endpgm\n"
  "Lcls1:\n"
  "s_waitcnt vmcnt(4)\n"
  "s_barrier\n"
  "s_lshl_b32 s91, s69, 7\n"
  "s_add_u32 m0, s37, 81920\n"
  "s_nop 0\n"
  "buffer_load_dwordx4 v2, s[4:7], s91 offen lds\n"
  "buffer_load_dwordx4 v3, s[4:7], s91 offen offset:1024 lds\n"
  "buffer_load_dwordx4 v4, s[4:7], s91 offen offset:2048 lds\n"
  "buffer_load_dwordx4 v5, s[4:7], s91 offen offset:3072 lds\n"
  "s_lshl_b32 s91, s69, 19\n"
  "s_add_u32 m0, s37, 32768\n"
  "s_nop 0\n"
  "buffer_load_dwordx4 v6, s[8:11], s91 offen lds\n"
  "buffer_load_dwordx4 v7, s[8:11], s91 offen offset:1024 lds\n"
  "buffer_load_dwordx4 v8, s[8:11], s91 offen offset:2048 lds\n"
  "buffer_load_dwordx4 v9, s[8:11], s91 offen offset:3072 lds\n"
  "s_waitcnt vmcnt(8)\n"
  "s_barrier\n"
  "s_lshl_b32 s91, s69, 19\n"
  "s_add_u32 s91, s91, 256\n"
  "s_add_u32 m0, s37, 49152\n"
  "s_nop 0\n"
  "buffer_load_dwordx4 v6, s[8:11], s91 offen lds\n"
  "buffer_load_dwordx4 v7, s[8:11], s91 offen offset:1024 lds\n"
  "buffer_load_dwordx4 v8, s[8:11], s91 offen offset:2048 lds\n"
  "buffer_load_dwordx4 v9, s[8:11], s91 offen offset:3072 lds\n"
  "s_lshl_b32 s91, s70, 7\n"
  "s_add_u32 m0, s37, 65536\n"
  "s_nop 0\n"
  "buffer_load_dwordx4 v2, s[4:7], s91 offen lds\n"
  "buffer_load_dwordx4 v3, s[4:7], s91 offen offset:1024 lds\n"
  "buffer_load_dwordx4 v4, s[4:7], s91 offen offset:2048 lds\n"
  "buffer_load_dwordx4 v5, s[4:7], s91 offen offset:3072 lds\n"
  "s_waitcnt vmcnt(8)\n"
  "s_barrier\n"
  "s_lshl_b32 s91, s70, 19\n"
  "s_add_u32 m0, s37, 0\n"
  "s_nop 0\n"
  "buffer_load_dwordx4 v6, s[8:11], s91 offen lds\n"
  "buffer_load_dwordx4 v7, s[8:11], s91 offen offset:1024 lds\n"
  "buffer_load_dwordx4 v8, s[8:11], s91 offen offset:2048 lds\n"
  "buffer_load_dwordx4 v9, s[8:11], s91 offen offset:3072 lds\n"
  "s_waitcnt vmcnt(8)\n"
  "s_barrier\n"
  "s_lshl_b32 s91, s70, 19\n"
  "s_add_u32 s91, s91, 256\n"
  "s_add_u32 m0, s37, 16384\n"
  "s_nop 0\n"
  "buffer_load_dwordx4 v6, s[8:11], s91 offen lds\n"
  "buffer_load_dwordx4 v7, s[8:11], s91 offen offset:1024 lds\n"
  "buffer_load_dwordx4 v8, s[8:11], s91 offen offset:2048 lds\n"
  "buffer_load_dwordx4 v9, s[8:11], s91 offen offset:3072 lds\n"
  "s_lshl_b32 s91, s71, 7\n"
  "s_add_u32 m0, s37, 81920\n"
  "s_nop 0\n"
  "buffer_load_dwordx4 v2, s[4:7], s91 offen lds\n"
  "buffer_load_dwordx4 v3, s[4:7], s91 offen offset:1024 lds\n"
  "buffer_load_dwordx4 v4, s[4:7], s91 offen offset:2048 lds\n"
  "buffer_load_dwordx4 v5, s[4:7], s91 offen offset:3072 lds\n"
  "s_waitcnt vmcnt(8)\n"
  "s_barrier\n"
  "s_lshl_b32 s91, s71, 19\n"
  "s_add_u32 m0, s37, 32768\n"
  "s_nop 0\n"
  "buffer_load_dwordx4 v6, s[8:11], s91 offen lds\n"
  "buffer_load_dwordx4 v7, s[8:11], s91 offen offset:1024 lds\n"
  "buffer_load_dwordx4 v8, s[8:11], s91 offen offset:2048 lds\n"
  "buffer_load_dwordx4 v9, s[8:11], s91 offen offset:3072 lds\n"
  "s_mov_b32 s91, 0\n"
  "s_add_u32 m0, s37, 98304\n"
  "s_nop 0\n"
  "buffer_load_dwordx4 v18, s[12:15], s91 offen nt lds\n"
  "buffer_load_dwordx4 v19, s[12:15], s91 offen offset:1024 nt lds\n"
  "buffer_load_dwordx4 v20, s[12:15], s91 offen offset:2048 nt lds\n"
  "buffer_load_dwordx4 v21, s[12:15], s91 offen offset:3072 nt lds\n"
  "s_mov_b32 s91, 524288\n"
  "s_add_u32 m0, s37, 114688\n"
  "s_nop 0\n"
  "buffer_load_dwordx4 v18, s[12:15], s91 offen nt lds\n"
  "buffer_load_dwordx4 v19, s[12:15], s91 offen offset:1024 nt lds\n"
  "buffer_load_dwordx4 v20, s[12:15], s91 offen offset:2048 nt lds\n"
  "buffer_load_dwordx4 v21, s[12:15], s91 offen offset:3072 nt lds\n"
  "s_waitcnt vmcnt(16)\n"
  "s_barrier\n"
  "s_lshl_b32 s91, s71, 19\n"
  "s_add_u32 s91, s91, 256\n"
  "s_add_u32 m0, s37, 49152\n"
  "s_nop 0\n"
  "buffer_load_dwordx4 v6, s[8:11], s91 offen lds\n"
  "buffer_load_dwordx4 v7, s[8:11], s91 offen offset:1024 lds\n"
  "buffer_load_dwordx4 v8, s[8:11], s91 offen offset:2048 lds\n"
  "buffer_load_dwordx4 v9, s[8:11], s91 offen offset:3072 lds\n"
  "s_lshl_b32 s91, s72, 7\n"
  "s_add_u32 m0, s37, 65536\n"
  "s_nop 0\n"
  "buffer_load_dwordx4 v2, s[4:7], s91 offen lds\n"
  "buffer_load_dwordx4 v3, s[4:7], s91 offen offset:1024 lds\n"
  "buffer_load_dwordx4 v4, s[4:7], s91 offen offset:2048 lds\n"
  "buffer_load_dwordx4 v5, s[4:7], s91 offen offset:3072 lds\n"
  "s_waitcnt vmcnt(16)\n"
  "s_barrier\n"
  "s_lshl_b32 s91, s72, 19\n"
  "s_add_u32 m0, s37, 0\n"
  "s_nop 0\n"
  "buffer_load_dwordx4 v6, s[8:11], s91 offen lds\n"
  "buffer_load_dwordx4 v7, s[8:11], s91 offen offset:1024 lds\n"
  "buffer_load_dwordx4 v8, s[8:11], s91 offen offset:2048 lds\n"
  "buffer_load_dwordx4 v9, s[8:11], s91 offen offset:3072 lds\n"
  "s_waitcnt vmcnt(8)\n"
  "s_barrier\n"
  "s_lshl_b32 s91, s72, 19\n"
  "s_add_u32 s91, s91, 256\n"
  "s_add_u32 m0, s37, 16384\n"
  "s_nop 0\n"
  "buffer_load_dwordx4 v6, s[8:11], s91 offen lds\n"
  "buffer_load_dwordx4 v7, s[8:11], s91 offen offset:1024 lds\n"
  "buffer_load_dwordx4 v8, s[8:11], s91 offen offset:2048 lds\n"
  "buffer_load_dwordx4 v9, s[8:11], s91 offen offset:3072 lds\n"
  "s_lshl_b32 s91, s73, 7\n"
  "s_add_u32 m0, s37, 81920\n"
  "s_nop 0\n"
  "buffer_load_dwordx4 v2, s[4:7], s91 offen lds\n"
  "buffer_load_dwordx4 v3, s[4:7], s91 offen offset:1024 lds\n"
  "buffer_load_dwordx4 v4, s[4:7], s91 offen offset:2048 lds\n"
  "buffer_load_dwordx4 v5, s[4:7], s91 offen offset:3072 lds\n"
  "s_waitcnt vmcnt(8)\n"
  "s_barrier\n"
  "s_lshl_b32 s91, s73, 19\n"
  "s_add_u32 m0, s37, 32768\n"
  "s_nop 0\n"
  "buffer_load_dwordx4 v6, s[8:11], s91 offen lds\n"
  "buffer_load_dwordx4 v7, s[8:11], s91 offen offset:1024 lds\n"
  "buffer_load_dwordx4 v8, s[8:11], s91 offen offset:2048 lds\n"
  "buffer_load_dwordx4 v9, s[8:11], s91 offen offset:3072 lds\n"
  "s_mov_b32 s91, 0\n"
  "s_add_u32 m0, s37, 131072\n"
  "s_nop 0\n"
  "buffer_load_dwordx4 v14, s[16:19], s91 offen nt lds\n"
  "buffer_load_dwordx4 v15, s[16:19], s91 offen offset:1024 nt lds\n"
  "buffer_load_dwordx4 v16, s[16:19], s91 offen offset:2048 nt lds\n"
  "buffer_load_dwordx4 v17, s[16:19], s91 offen offset:3072 nt lds\n"
  "s_mov_b32 s91, 524288\n"
  "s_add_u32 m0, s37, 147456\n"
  "s_nop 0\n"
  "buffer_load_dwordx4 v14, s[16:19], s91 offen nt lds\n"
  "buffer_load_dwordx4 v15, s[16:19], s91 offen offset:1024 nt lds\n"
  "buffer_load_dwordx4 v16, s[16:19], s91 offen offset:2048 nt lds\n"
  "buffer_load_dwordx4 v17, s[16:19], s91 offen offset:3072 nt lds\n"
  "s_waitcnt lgkmcnt(0)\n"
  "s_add_u32 s46, s50, 0x9e3779b9\n"
  "s_add_u32 s47, s51, 0x7f4a7c15\n"
  "s_add_u32 s50, s50, 1\n"
  "v_mov_b32 v26, s50\n"
  "v_mov_b32 v27, s51\n"
  "s_waitcnt vmcnt(16)\n"
  "s_barrier\n"
  "s_lshl_b32 s91, s73, 19\n"
  "s_add_u32 s91, s91, 256\n"
  "s_add_u32 m0, s37, 49152\n"
  "s_nop 0\n"
  "buffer_load_dwordx4 v6, s[8:11], s91 offen lds\n"
  "buffer_load_dwordx4 v7, s[8:11], s91 offen offset:1024 lds\n"
  "buffer_load_dwordx4 v8, s[8:11], s91 offen offset:2048 lds\n"
  "buffer_load_dwordx4 v9, s[8:11], s91 offen offset:3072 lds\n"
  "s_lshl_b32 s91, s74, 7\n"
  "s_add_u32 m0, s37, 65536\n"
  "s_nop 0\n"
  "buffer_load_dwordx4 v2, s[4:7], s91 offen lds\n"
  "buffer_load_dwordx4 v3, s[4:7], s91 offen offset:1024 lds\n"
  "buffer_load_dwordx4 v4, s[4:7], s91 offen offset:2048 lds\n"
  "buffer_load_dwordx4 v5, s[4:7], s91 offen offset:3072 lds\n"
  "s_waitcnt vmcnt(16)\n"
  "s_barrier\n"
  "s_lshl_b32 s91, s74, 19\n"
  "s_add_u32 m0, s37, 0\n"
  "s_nop 0\n"
  "buffer_load_dwordx4 v6, s[8:11], s91 offen lds\n"
  "buffer_load_dwordx4 v7, s[8:11], s91 offen offset:1024 lds\n"
  "buffer_load_dwordx4 v8, s[8:11], s91 offen offset:2048 lds\n"
  "buffer_load_dwordx4 v9, s[8:11], s91 offen offset:3072 lds\n"
  "s_waitcnt vmcnt(8)\n"
  "s_barrier\n"
  "s_lshl_b32 s91, s75, 19\n"
  "s_add_u32 m0, s37, 16384\n"
  "s_nop 0\n"
  "buffer_load_dwordx4 v6, s[8:11], s91 offen lds\n"
  "buffer_load_dwordx4 v7, s[8:11], s91 offen offset:1024 lds\n"
  "buffer_load_dwordx4 v8, s[8:11], s91 offen offset:2048 lds\n"
  "buffer_load_dwordx4 v9, s[8:11], s91 offen offset:3072 lds\n"
  "s_lshl_b32 s91, s75, 7\n"
  "s_add_u32 m0, s37, 81920\n"
  "s_nop 0\n"
  "buffer_load_dwordx4 v2, s[4:7], s91 offen lds\n"
  "buffer_load_dwordx4 v3, s[4:7], s91 offen offset:1024 lds\n"
  "buffer_load_dwordx4 v4, s[4:7], s91 offen offset:2048 lds\n"
  "buffer_load_dwordx4 v5, s[4:7], s91 offen offset:3072 lds\n"
  "s_waitcnt vmcnt(8)\n"
  "s_barrier\n"
  "s_lshl_b32 s91, s74, 19\n"
  "s_add_u32 s91, s91, 256\n"
  "s_add_u32 m0, s37, 32768\n"
  "s_nop 0\n"
  "buffer_load_dwordx4 v6, s[8:11], s91 offen lds\n"
  "buffer_load_dwordx4 v7, s[8:11], s91 offen offset:1024 lds\n"
  "buffer_load_dwordx4 v8, s[8:11], s91 offen offset:2048 lds\n"
  "buffer_load_dwordx4 v9, s[8:11], s91 offen offset:3072 lds\n"
  "s_mov_b32 s91, 16384\n"
  "buffer_load_dwordx4 v[104:107], v18, s[12:15], s91 offen nt\n"
  "buffer_load_dwordx4 v[108:111], v19, s[12:15], s91 offen offset:1024 nt\n"
  "buffer_load_dwordx4 v[112:115], v20, s[12:15], s91 offen offset:2048 nt\n"
  "buffer_load_dwordx4 v[116:119], v21, s[12:15], s91 offen offset:3072 nt\n"
  "s_mov_b32 s91, 540672\n"
  "buffer_load_dwordx4 v[120:123], v18, s[12:15], s91 offen nt\n"
  "buffer_load_dwordx4 v[124:127], v19, s[12:15], s91 offen offset:1024 nt\n"
  "buffer_load_dwordx4 v[128:131], v20, s[12:15], s91 offen offset:2048 nt\n"
  "buffer_load_dwordx4 v[132:135], v21, s[12:15], s91 offen offset:3072 nt\n"
  "s_waitcnt vmcnt(12)\n"
  "s_barrier\n"
  "s_lshl_b32 s91, s75, 19\n"
  "s_add_u32 s91, s91, 256\n"
  "s_add_u32 m0, s37, 49152\n"
  "s_nop 0\n"
  "buffer_load_dwordx4 v6, s[8:11], s91 offen lds\n"
  "buffer_load_dwordx4 v7, s[8:11], s91 offen offset:1024 lds\n"
  "buffer_load_dwordx4 v8, s[8:11], s91 offen offset:2048 lds\n"
  "buffer_load_dwordx4 v9, s[8:11], s91 offen offset:3072 lds\n"
  "s_barrier\n"
  "s_mov_b32 s91, 16384\n"
  "buffer_load_dwordx4 v[136:139], v14, s[16:19], s91 offen nt\n"
  "buffer_load_dwordx4 v[140:143], v15, s[16:19], s91 offen offset:1024 nt\n"
  "buffer_load_dwordx4 v[144:147], v16, s[16:19], s91 offen offset:2048 nt\n"
  "buffer_load_dwordx4 v[148:151], v17, s[16:19], s91 offen offset:3072 nt\n"
  "s_mov_b32 s91, 540672\n"
  "buffer_load_dwordx4 v[152:155], v14, s[16:19], s91 offen nt\n"
  "buffer_load_dwordx4 v[156:159], v15, s[16:19], s91 offen offset:1024 nt\n"
  "buffer_load_dwordx4 v[160:163], v16, s[16:19], s91 offen offset:2048 nt\n"
  "buffer_load_dwordx4 v[164:167], v17, s[16:19], s91 offen offset:3072 nt\n"
  "s_barrier\n"
  "s_barrier\n"
  "s_waitcnt vmcnt(16)\n"
  "ds_write_b128 v36, v[104:107] offset:0\n"
  "ds_write_b128 v36, v[108:111] offset:1024\n"
  "ds_write_b128 v36, v[112:115] offset:2048\n"
  "ds_write_b128 v36, v[116:119] offset:3072\n"
  "s_waitcnt vmcnt(12)\n"
  "ds_write_b128 v36, v[120:123] offset:16384\n"
  "ds_write_b128 v36, v[124:127] offset:17408\n"
  "ds_write_b128 v36, v[128:131] offset:18432\n"
  "ds_write_b128 v36, v[132:135] offset:19456\n"
  "s_waitcnt lgkmcnt(0)\n"
  "s_barrier\n"
  "s_barrier\n"
  "s_waitcnt vmcnt(8)\n"
  "s_barrier\n"
  "s_add_u32 s91, s66, 0\n"
  "s_add_u32 m0, s37, 131072\n"
  "s_nop 0\n"
  "buffer_load_dwordx4 v10, s[20:23], s91 offen lds\n"
  "buffer_load_dwordx4 v11, s[20:23], s91 offen offset:1024 lds\n"
  "buffer_load_dwordx4 v12, s[20:23], s91 offen offset:2048 lds\n"
  "buffer_load_dwordx4 v13, s[20:23], s91 offen offset:3072 lds\n"
  "s_add_u32 s91, s66, 65536\n"
  "s_add_u32 m0, s37, 147456\n"
  "s_nop 0\n"
  "buffer_load_dwordx4 v10, s[20:23], s91 offen lds\n"
  "buffer_load_dwordx4 v11, s[20:23], s91 offen offset:1024 lds\n"
  "buffer_load_dwordx4 v12, s[20:23], s91 offen offset:2048 lds\n"
  "buffer_load_dwordx4 v13, s[20:23], s91 offen offset:3072 lds\n"
  "s_barrier\n"
  "v_add_u32 v36, 65536, v36\n"
  "s_waitcnt vmcnt(12)\n"
  "ds_write_b128 v36, v[136:139] offset:32768\n"
  "ds_write_b128 v36, v[140:143] offset:33792\n"
  "ds_write_b128 v36, v[144:147] offset:34816\n"
  "ds_write_b128 v36, v[148:151] offset:35840\n"
  "s_add_u32 s91, s66, 131072\n"
  "s_add_u32 m0, s37, 32768\n"
  "s_nop 0\n"
  "buffer_load_dwordx4 v10, s[20:23], s91 offen lds\n"
  "buffer_load_dwordx4 v11, s[20:23], s91 offen offset:1024 lds\n"
  "buffer_load_dwordx4 v12, s[20:23], s91 offen offset:2048 lds\n"
  "buffer_load_dwordx4 v13, s[20:23], s91 offen offset:3072 lds\n"
  "s_waitcnt lgkmcnt(0)\n"
  "s_barrier\n"
  "s_waitcnt vmcnt(12)\n"
  "ds_write_b128 v36, v[152:155] offset:49152\n"
  "ds_write_b128 v36, v[156:159] offset:50176\n"
  "ds_write_b128 v36, v[160:163] offset:51200\n"
  "ds_write_b128 v36, v[164:167] offset:52224\n"
  "s_add_u32 s91, s66, 196608\n"
  "s_add_u32 m0, s37, 49152\n"
  "s_nop 0\n"
  "buffer_load_dwordx4 v10, s[20:23], s91 offen lds\n"
  "buffer_load_dwordx4 v11, s[20:23], s91 offen offset:1024 lds\n"
  "buffer_load_dwordx4 v12, s[20:23], s91 offen offset:2048 lds\n"
  "buffer_load_dwordx4 v13, s[20:23], s91 offen offset:3072 lds\n"
  "s_waitcnt lgkmcnt(0)\n"
  "s_barrier\n"
  "s_add_u32 s91, s54, 0\n"
  "s_add_u32 m0, s37, 0\n"
  "s_nop 0\n"
  "buffer_load_dwordx4 v10, s[20:23], s91 offen lds\n"
  "buffer_load_dwordx4 v11, s[20:23], s91 offen offset:1024 lds\n"
  "buffer_load_dwordx4 v12, s[20:23], s91 offen offset:2048 lds\n"
  "buffer_load_dwordx4 v13, s[20:23], s91 offen offset:3072 lds\n"
  "s_add_u32 s91, s55, 0\n"
  "s_add_u32 m0, s37, 16384\n"
  "s_nop 0\n"
  "buffer_load_dwordx4 v10, s[20:23], s91 offen lds\n"
  "buffer_load_dwordx4 v11, s[20:23], s91 offen offset:1024 lds\n"
  "buffer_load_dwordx4 v12, s[20:23], s91 offen offset:2048 lds\n"
  "buffer_load_dwordx4 v13, s[20:23], s91 offen offset:3072 lds\n"
  "s_barrier\n"
  "buffer_load_dwordx2 v[24:25], v23, s[24:27], s68 offen sc1\n"
  "s_waitcnt vmcnt(0)\n"
  "v_cmp_ne_u64 vcc, s[46:47], v[24:25]\n"
  "s_cbranch_vccnz Lslow_o0c1\n"
  "Lback_o0c1:\n"
  "s_barrier\n"
  "s_barrier\n"
  "s_add_u32 s91, s60, 0\n"
  "s_add_u32 m0, s37, 65536\n"
  "s_nop 0\n"
  "buffer_load_dwordx4 v22, s[24:27], s91 offen sc1 lds\n"
  "buffer_load_dwordx4 v22, s[24:27], s91 offen offset:1024 sc1 lds\n"
  "buffer_load_dwordx4 v22, s[24:27], s91 offen offset:2048 sc1 lds\n"
  "buffer_load_dwordx4 v22, s[24:27], s91 offen offset:3072 sc1 lds\n"
  "s_add_u32 s91, s61, 0\n"
  "s_add_u32 m0, s37, 81920\n"
  "s_nop 0\n"
  "buffer_load_dwordx4 v22, s[24:27], s91 offen sc1 lds\n"
  "buffer_load_dwordx4 v22, s[24:27], s91 offen offset:1024 sc1 lds\n"
  "buffer_load_dwordx4 v22, s[24:27], s91 offen offset:2048 sc1 lds\n"
  "buffer_load_dwordx4 v22, s[24:27], s91 offen offset:3072 sc1 lds\n"
  "s_barrier\n"
  "s_add_u32 s91, s56, 0\n"
  "s_add_u32 m0, s37, 131072\n"
  "s_nop 0\n"
  "buffer_load_dwordx4 v10, s[20:23], s91 offen lds\n"
  "buffer_load_dwordx4 v11, s[20:23], s91 offen offset:1024 lds\n"
  "buffer_load_dwordx4 v12, s[20:23], s91 offen offset:2048 lds\n"
  "buffer_load_dwordx4 v13, s[20:23], s91 offen offset:3072 lds\n"
  "s_mov_b64 exec, 1\n"
  "buffer_store_dwordx2 v[26:27], v28, s[24:27], s78 offen sc1\n"
  "s_mov_b64 exec, -1\n"
  "s_barrier\n"
  "s_add_u32 s91, s62, 0\n"
  "s_add_u32 m0, s37, 98304\n"
  "s_nop 0\n"
  "buffer_load_dwordx4 v22, s[24:27], s91 offen sc1 lds\n"
  "buffer_load_dwordx4 v22, s[24:27], s91 offen offset:1024 sc1 lds\n"
  "buffer_load_dwordx4 v22, s[24:27], s91 offen offset:2048 sc1 lds\n"
  "buffer_load_dwordx4 v22, s[24:27], s91 offen offset:3072 sc1 lds\n"
  "s_add_u32 s91, s57, 0\n"
  "s_add_u32 m0, s37, 147456\n"
  "s_nop 0\n"
  "buffer_load_dwordx4 v10, s[20:23], s91 offen lds\n"
  "buffer_load_dwordx4 v11, s[20:23], s91 offen offset:1024 lds\n"
  "buffer_load_dwordx4 v12, s[20:23], s91 offen offset:2048 lds\n"
  "buffer_load_dwordx4 v13, s[20:23], s91 offen offset:3072 lds\n"
  "s_waitcnt vmcnt(17)\n"
  "s_barrier\n"
  "s_add_u32 s91, s63, 0\n"
  "s_add_u32 m0, s37, 114688\n"
  "s_nop 0\n"
  "buffer_load_dwordx4 v22, s[24:27], s91 offen sc1 lds\n"
  "buffer_load_dwordx4 v22, s[24:27], s91 offen offset:1024 sc1 lds\n"
  "buffer_load_dwordx4 v22, s[24:27], s91 offen offset:2048 sc1 lds\n"
  "buffer_load_dwordx4 v22, s[24:27], s91 offen offset:3072 sc1 lds\n"
  "s_add_u32 s91, s58, 0\n"
  "s_add_u32 m0, s37, 32768\n"
  "s_nop 0\n"
  "buffer_load_dwordx4 v10, s[20:23], s91 offen lds\n"
  "buffer_load_dwordx4 v11, s[20:23], s91 offen offset:1024 lds\n"
  "buffer_load_dwordx4 v12, s[20:23], s91 offen offset:2048 lds\n"
  "buffer_load_dwordx4 v13, s[20:23], s91 offen offset:3072 lds\n"
  "s_waitcnt vmcnt(21)\n"
  "s_barrier\n"
  "s_add_u32 s91, s64, 0\n"
  "s_add_u32 m0, s37, 65536\n"
  "s_nop 0\n"
  "buffer_load_dwordx4 v22, s[24:27], s91 offen sc1 lds\n"
  "buffer_load_dwordx4 v22, s[24:27], s91 offen offset:1024 sc1 lds\n"
  "buffer_load_dwordx4 v22, s[24:27], s91 offen offset:2048 sc1 lds\n"
  "buffer_load_dwordx4 v22, s[24:27], s91 offen offset:3072 sc1 lds\n"
  "s_add_u32 s91, s59, 0\n"
  "s_add_u32 m0, s37, 49152\n"
  "s_nop 0\n"
  "buffer_load_dwordx4 v10, s[20:23], s91 offen lds\n"
  "buffer_load_dwordx4 v11, s[20:23], s91 offen offset:1024 lds\n"
  "buffer_load_dwordx4 v12, s[20:23], s91 offen offset:2048 lds\n"
  "buffer_load_dwordx4 v13, s[20:23], s91 offen offset:3072 lds\n"
  "s_waitcnt vmcnt(20)\n"
  "s_barrier\n"
  "buffer_load_dwordx2 v[24:25], v23, s[24:27], s68 offen offset:8 sc1\n"
  "s_add_u32 s91, s65, 0\n"
  "s_add_u32 m0, s37, 81920\n"
  "s_nop 0\n"
  "buffer_load_dwordx4 v22, s[24:27], s91 offen sc1 lds\n"
  "buffer_load_dwordx4 v22, s[24:27], s91 offen offset:1024 sc1 lds\n"
  "buffer_load_dwordx4 v22, s[24:27], s91 offen offset:2048 sc1 lds\n"
  "buffer_load_dwordx4 v22, s[24:27], s91 offen offset:3072 sc1 lds\n"
  "s_add_u32 s91, s54, 131072\n"
  "s_add_u32 m0, s37, 0\n"
  "s_nop 0\n"
  "buffer_load_dwordx4 v10, s[20:23], s91 offen lds\n"
  "buffer_load_dwordx4 v11, s[20:23], s91 offen offset:1024 lds\n"
  "buffer_load_dwordx4 v12, s[20:23], s91 offen offset:2048 lds\n"
  "buffer_load_dwordx4 v13, s[20:23], s91 offen offset:3072 lds\n"
  "s_waitcnt vmcnt(8)\n"
  "v_cmp_ne_u64 vcc, s[46:47], v[24:25]\n"
  "s_cbranch_vccnz Lslow_o1c1\n"
  "Lback_o1c1:\n"
  "s_barrier\n"
  "s_add_u32 s91, s60, 32768\n"
  "s_add_u32 m0, s37, 98304\n"
  "s_nop 0\n"
  "buffer_load_dwordx4 v22, s[24:27], s91 offen sc1 lds\n"
  "buffer_load_dwordx4 v22, s[24:27], s91 offen offset:1024 sc1 lds\n"
  "buffer_load_dwordx4 v22, s[24:27], s91 offen offset:2048 sc1 lds\n"
  "buffer_load_dwordx4 v22, s[24:27], s91 offen offset:3072 sc1 lds\n"
  "s_add_u32 s91, s55, 131072\n"
  "s_add_u32 m0, s37, 16384\n"
  "s_nop 0\n"
  "buffer_load_dwordx4 v10, s[20:23], s91 offen lds\n"
  "buffer_load_dwordx4 v11, s[20:23], s91 offen offset:1024 lds\n"
  "buffer_load_dwordx4 v12, s[20:23], s91 offen offset:2048 lds\n"
  "buffer_load_dwordx4 v13, s[20:23], s91 offen offset:3072 lds\n"
  "s_barrier\n"
  "s_add_u32 s91, s61, 32768\n"
  "s_add_u32 m0, s37, 114688\n"
  "s_nop 0\n"
  "buffer_load_dwordx4 v22, s[24:27], s91 offen sc1 lds\n"
  "buffer_load_dwordx4 v22, s[24:27], s91 offen offset:1024 sc1 lds\n"
  "buffer_load_dwordx4 v22, s[24:27], s91 offen offset:2048 sc1 lds\n"
  "buffer_load_dwordx4 v22, s[24:27], s91 offen offset:3072 sc1 lds\n"
  "s_add_u32 s91, s56, 131072\n"
  "s_add_u32 m0, s37, 131072\n"
  "s_nop 0\n"
  "buffer_load_dwordx4 v10, s[20:23], s91 offen lds\n"
  "buffer_load_dwordx4 v11, s[20:23], s91 offen offset:1024 lds\n"
  "buffer_load_dwordx4 v12, s[20:23], s91 offen offset:2048 lds\n"
  "buffer_load_dwordx4 v13, s[20:23], s91 offen offset:3072 lds\n"
  "s_waitcnt vmcnt(20)\n"
  "s_barrier\n"
  "s_add_u32 s91, s62, 32768\n"
  "s_add_u32 m0, s37, 65536\n"
  "s_nop 0\n"
  "buffer_load_dwordx4 v22, s[24:27], s91 offen sc1 lds\n"
  "buffer_load_dwordx4 v22, s[24:27], s91 offen offset:1024 sc1 lds\n"
  "buffer_load_dwordx4 v22, s[24:27], s91 offen offset:2048 sc1 lds\n"
  "buffer_load_dwordx4 v22, s[24:27], s91 offen offset:3072 sc1 lds\n"
  "s_add_u32 s91, s57, 131072\n"
  "s_add_u32 m0, s37, 147456\n"
  "s_nop 0\n"
  "buffer_load_dwordx4 v10, s[20:23], s91 offen lds\n"
  "buffer_load_dwordx4 v11, s[20:23], s91 offen offset:1024 lds\n"
  "buffer_load_dwordx4 v12, s[20:23], s91 offen offset:2048 lds\n"
  "buffer_load_dwordx4 v13, s[20:23], s91 offen offset:3072 lds\n"
  "s_waitcnt vmcnt(20)\n"
  "s_barrier\n"
  "s_add_u32 s91, s63, 32768\n"
  "s_add_u32 m0, s37, 81920\n"
  "s_nop 0\n"
  "buffer_load_dwordx4 v22, s[24:27], s91 offen sc1 lds\n"
  "buffer_load_dwordx4 v22, s[24:27], s91 offen offset:1024 sc1 lds\n"
  "buffer_load_dwordx4 v22, s[24:27], s91 offen offset:2048 sc1 lds\n"
  "buffer_load_dwordx4 v22, s[24:27], s91 offen offset:3072 sc1 lds\n"
  "s_add_u32 s91, s58, 131072\n"
  "s_add_u32 m0, s37, 32768\n"
  "s_nop 0\n"
  "buffer_load_dwordx4 v10, s[20:23], s91 offen lds\n"
  "buffer_load_dwordx4 v11, s[20:23], s91 offen offset:1024 lds\n"
  "buffer_load_dwordx4 v12, s[20:23], s91 offen offset:2048 lds\n"
  "buffer_load_dwordx4 v13, s[20:23], s91 offen offset:3072 lds\n"
  "s_waitcnt vmcnt(20)\n"
  "s_barrier\n"
  "s_add_u32 s91, s64, 32768\n"
  "s_add_u32 m0, s37, 98304\n"
  "s_nop 0\n"
  "buffer_load_dwordx4 v22, s[24:27], s91 offen sc1 lds\n"
  "buffer_load_dwordx4 v22, s[24:27], s91 offen offset:1024 sc1 lds\n"
  "buffer_load_dwordx4 v22, s[24:27], s91 offen offset:2048 sc1 lds\n"
  "buffer_load_dwordx4 v22, s[24:27], s91 offen offset:3072 sc1 lds\n"
  "s_add_u32 s91, s59, 131072\n"
  "s_add_u32 m0, s37, 49152\n"
  "s_nop 0\n"
  "buffer_load_dwordx4 v10, s[20:23], s91 offen lds\n"
  "buffer_load_dwordx4 v11, s[20:23], s91 offen offset:1024 lds\n"
  "buffer_load_dwordx4 v12, s[20:23], s91 offen offset:2048 lds\n"
  "buffer_load_dwordx4 v13, s[20:23], s91 offen offset:3072 lds\n"
  "s_waitcnt vmcnt(20)\n"
  "s_barrier\n"
  "s_add_u32 s91, s65, 32768\n"
  "s_add_u32 m0, s37, 114688\n"
  "s_nop 0\n"
  "buffer_load_dwordx4 v22, s[24:27], s91 offen sc1 lds\n"
  "buffer_load_dwordx4 v22, s[24:27], s91 offen offset:1024 sc1 lds\n"
  "buffer_load_dwordx4 v22, s[24:27], s91 offen offset:2048 sc1 lds\n"
  "buffer_load_dwordx4 v22, s[24:27], s91 offen offset:3072 sc1 lds\n"
  "s_waitcnt vmcnt(16)\n"
  "s_barrier\n"
  "s_waitcnt vmcnt(8)\n"
  "s_barrier\n"
  "s_waitcnt vmcnt(0)\n"
  "s_barrier\n"
  "s_endpgm\n"
  "Lcompute:\n"
  "s_setprio 1\n"
  "s_lshl_b32 s95, s33, 3\n"
  "s_add_u32 s95, s95, s35\n"
  "s_lshr_b32 s93, s37, 5\n"
  "s_lshl_b32 s78, s95, 9\n"
  "s_add_u32 s78, s78, s93\n"
  "s_add_u32 s78, s78, 25427968\n"
  "s_lshl_b32 s68, s95, 11\n"
  "s_add_u32 s68, s68, s93\n"
  "s_add_u32 s68, s68, 25165824\n"
  "s_lshl_b32 s94, s95, 18\n"
  "s_lshl_b32 s44, s37, 1\n"
  "s_add_u32 s94, s94, s44\n"
  "s_and_b32 s36, s33, 4\n"
  "s_add_u32 s36, s36, s34\n"
  "s_lshl_b32 s36, s36, 3\n"
  "s_add_u32 s36, s36, s35\n"
  "s_lshl_b32 s79, s36, 9\n"
  "s_add_u32 s79, s79, s93\n"
  "s_add_u32 s79, s79, 25493504\n"
  "s_lshl_b32 s80, s75, 2\n"
  "s_add_u32 s80, s80, s34\n"
  "s_lshl_b32 s80, s80, 2\n"
  "s_add_u32 s80, s80, s92\n"
  "s_lshl_b32 s80, s80, 6\n"
  "s_lshl_b32 s83, s35, 18\n"
  "s_add_u32 s80, s80, s83\n"
  "s_sub_u32 s30, 0x200000, s80\n"
  "s_mov_b32 s31, 0x20000\n"
  "s_waitcnt lgkmcnt(0)\n"
  "s_add_u32 s28, s96, s80\n"
  "s_addc_u32 s29, s97, 0\n"
  "s_and_b32 s29, s29, 0xffff\n"
  "s_mov_b32 s24, s98\n"
  "s_and_b32 s25, s99, 0xffff\n"
  "s_load_dwordx2 s[50:51], s[24:25], s78\n"
  "s_load_dwordx2 s[52:53], s[24:25], s79\n"
  "v_lshlrev_b32 v1, 4, v0\n"
  "v_lshrrev_b32 v16, 4, v0\n"
  "v_bfe_u32 v17, v0, 2, 2\n"
  "v_bfe_u32 v18, v0, 3, 2\n"
  "v_and_b32 v19, 3, v0\n"
  "v_lshlrev_b32 v19, 3, v19\n"
  "v_lshl_add_u32 v19, v18, 5, v19\n"
  "v_lshl_add_u32 v19, v17, 7, v19\n"
  "v_lshl_add_u32 v2, v16, 10, v19\n"
  "v_lshl_add_u32 v6, v16, 9, v19\n"
  "v_xor_b32 v3, 32, v2\n"
  "v_xor_b32 v7, 32, v6\n"
  "v_xor_b32 v4, 64, v2\n"
  "v_xor_b32 v8, 64, v6\n"
  "v_xor_b32 v5, 96, v2\n"
  "v_xor_b32 v9, 96, v6\n"
  "v_and_b32 v17, 15, v0\n"
  "v_bfe_u32 v18, v0, 1, 3\n"
  "v_xor_b32 v18, v18, v16\n"
  "v_lshlrev_b32 v18, 4, v18\n"
  "v_lshl_add_u32 v18, v17, 7, v18\n"
  "v_add_u32 v10, s37, v18\n"
  "v_xor_b32 v11, 64, v10\n"
  "v_add_u32 v12, s37, v1\n"
  "v_lshrrev_b32 v18, 5, v0\n"
  "v_xor_b32 v18, v18, v17\n"
  "v_lshlrev_b32 v18, 4, v18\n"
  "v_lshl_add_u32 v18, v17, 8, v18\n"
  "v_and_b32 v19, 1, v16\n"
  "v_lshl_add_u32 v18, v19, 3, v18\n"
  "s_lshr_b32 s42, s37, 13\n"
  "s_lshl_b32 s38, s42, 14\n"
  "v_add_u32 v13, s38, v18\n"
  "v_mov_b32 v24, 0\n"
  "s_lshl_b32 s69, s34, 9\n"
  "s_add_u32 s69, s69, s68\n"
  "s_lshl_b32 s70, s36, 11\n"
  "s_add_u32 s70, s70, s93\n"
  "s_add_u32 s70, s70, 25296896\n"
  "s_lshl_b32 s71, s92, 9\n"
  "s_add_u32 s71, s71, s70\n"
  "s_lshl_b32 s42, s36, 17\n"
  "s_lshr_b32 s44, s37, 1\n"
  "s_add_u32 s42, s42, s44\n"
  "s_add_u32 s42, s42, 16777216\n"
  "s_lshl_b32 s43, s92, 13\n"
  "s_add_u32 s43, s43, s42\n"
  "s_lshl_b32 s44, s92, 15\n"
  "s_add_u32 s44, s44, s42\n"
  "s_xor_b32 s45, s92, 3\n"
  "s_lshl_b32 s72, s45, 15\n"
  "s_add_u32 s72, s72, s43\n"
  "s_lshl_b32 s75, s45, 13\n"
  "s_add_u32 s75, s75, s44\n"
  "s_xor_b32 s45, s92, 2\n"
  "s_lshl_b32 s73, s45, 15\n"
  "s_add_u32 s73, s73, s43\n"
  "s_lshl_b32 s76, s45, 13\n"
  "s_add_u32 s76, s76, s44\n"
  "s_xor_b32 s45, s92, 1\n"
  "s_lshl_b32 s74, s45, 15\n"
  "s_add_u32 s74, s74, s43\n"
  "s_lshl_b32 s77, s45, 13\n"
  "s_add_u32 s77, s77, s44\n"
  "s_lshl_b32 s67, s34, 16\n"
  "s_add_u32 s67, s67, s94\n"
  "s_mov_b32 s54, 0x3e0293ee\n"
  "s_mov_b32 s55, 0x3e0293ee\n"
  "s_waitcnt lgkmcnt(0)\n"
  "s_add_u32 s46, s50, 0x9e3779b9\n"
  "s_add_u32 s47, s51, 0x7f4a7c15\n"
  "s_add_u32 s50, s50, 1\n"
  "s_add_u32 s48, s52, 0x9e3779b9\n"
  "s_add_u32 s49, s53, 0x7f4a7c15\n"
  "s_add_u32 s52, s52, 1\n"
  "v_mov_b32 v26, s46\n"
  "s_waitcnt vmcnt(0)\n"
  "v_mov_b32 v27, s47\n"
  "s_barrier\n"
  "ds_read_b64_tr_b16 v[28:29], v2 offset:0\n"
  "ds_read_b64_tr_b16 v[30:31], v2 offset:512\n"
  "v_add_u32 v10, 65536, v10\n"
  "v_add_u32 v11, 65536, v11\n"
  "ds_read_b128 v[48:51], v10 offset:0\n"
  "ds_read_b128 v[52:55], v10 offset:2048\n"
  "ds_read_b128 v[56:59], v11 offset:0\n"
  "ds_read_b128 v[60:63], v11 offset:2048\n"
  "ds_read_b64_tr_b16 v[32:33], v3 offset:0\n"
  "ds_read_b64_tr_b16 v[34:35], v3 offset:512\n"
  "ds_read_b64_tr_b16 v[36:37], v4 offset:0\n"
  "ds_read_b64_tr_b16 v[38:39], v4 offset:512\n"
  "ds_read_b64_tr_b16 v[40:41], v5 offset:0\n"
  "ds_read_b64_tr_b16 v[42:43], v5 offset:512\n"
  "s_waitcnt lgkmcnt(9)\n"
  "v_mfma_f32_16x16x32_bf16 v[64:67], v[28:31], v[48:51], 0\n"
  "ds_read_b64_tr_b16 v[44:45], v2 offset:8192\n"
  "s_waitcnt lgkmcnt(9)\n"
  "v_mfma_f32_16x16x32_bf16 v[68:71], v[28:31], v[52:55], 0\n"
  "ds_read_b64_tr_b16 v[46:47], v2 offset:8704\n"
  "s_waitcnt lgkmcnt(6)\n"
  "v_mfma_f32_16x16x32_bf16 v[76:79], v[32:35], v[52:55], 0\n"
  "ds_read_b64_tr_b16 v[28:29], v3 offset:8192\n"
  "v_mfma_f32_16x16x32_bf16 v[72:75], v[32:35], v[48:51], 0\n"
  "ds_read_b64_tr_b16 v[30:31], v3 offset:8704\n"
  "s_waitcnt lgkmcnt(6)\n"
  "v_mfma_f32_16x16x32_bf16 v[80:83], v[36:39], v[48:51], 0\n"
  "ds_read_b64_tr_b16 v[32:33], v4 offset:8192\n"
  "v_mfma_f32_16x16x32_bf16 v[84:87], v[36:39], v[52:55], 0\n"
  "ds_read_b64_tr_b16 v[34:35], v4 offset:8704\n"
  "s_waitcnt lgkmcnt(6)\n"
  "v_mfma_f32_16x16x32_bf16 v[92:95], v[40:43], v[52:55], 0\n"
  "ds_read_b64_tr_b16 v[36:37], v5 offset:8192\n"
  "v_mfma_f32_16x16x32_bf16 v[88:91], v[40:43], v[48:51], 0\n"
  "ds_read_b64_tr_b16 v[38:39], v5 offset:8704\n"
  "s_waitcnt lgkmcnt(6)\n"
  "v_mfma_f32_16x16x32_bf16 v[96:99], v[44:47], v[48:51], 0\n"
  "ds_read_b64_tr_b16 v[40:41], v2 offset:4096\n"
  "v_mfma_f32_16x16x32_bf16 v[100:103], v[44:47], v[52:55], 0\n"
  "ds_read_b64_tr_b16 v[42:43], v2 offset:4608\n"
  "s_waitcnt lgkmcnt(6)\n"
  "v_mfma_f32_16x16x32_bf16 v[108:111], v[28:31], v[52:55], 0\n"
  "ds_read_b64_tr_b16 v[44:45], v3 offset:4096\n"
  "v_mfma_f32_16x16x32_bf16 v[104:107], v[28:31], v[48:51], 0\n"
  "ds_read_b64_tr_b16 v[46:47], v3 offset:4608\n"
  "s_waitcnt lgkmcnt(6)\n"
  "v_mfma_f32_16x16x32_bf16 v[112:115], v[32:35], v[48:51], 0\n"
  "ds_read_b64_tr_b16 v[28:29], v4 offset:4096\n"
  "v_mfma_f32_16x16x32_bf16 v[116:119], v[32:35], v[52:55], 0\n"
  "ds_read_b64_tr_b16 v[30:31], v4 offset:4608\n"
  "s_waitcnt lgkmcnt(6)\n"
  "v_mfma_f32_16x16x32_bf16 v[124:127], v[36:39], v[52:55], 0\n"
  "ds_read_b64_tr_b16 v[32:33], v5 offset:4096\n"
  "v_mfma_f32_16x16x32_bf16 v[120:123], v[36:39], v[48:51], 0\n"
  "ds_read_b64_tr_b16 v[34:35], v5 offset:4608\n"
  "s_waitcnt lgkmcnt(6)\n"
  "v_mfma_f32_16x16x32_bf16 v[64:67], v[40:43], v[56:59], v[64:67]\n"
  "ds_read_b64_tr_b16 v[36:37], v2 offset:12288\n"
  "v_mfma_f32_16x16x32_bf16 v[68:71], v[40:43], v[60:63], v[68:71]\n"
  "ds_read_b64_tr_b16 v[38:39], v2 offset:12800\n"
  "s_waitcnt lgkmcnt(6)\n"
  "v_mfma_f32_16x16x32_bf16 v[76:79], v[44:47], v[60:63], v[76:79]\n"
  "ds_read_b64_tr_b16 v[40:41], v3 offset:12288\n"
  "v_mfma_f32_16x16x32_bf16 v[72:75], v[44:47], v[56:59], v[72:75]\n"
  "ds_read_b64_tr_b16 v[42:43], v3 offset:12800\n"
  "s_waitcnt lgkmcnt(6)\n"
  "v_mfma_f32_16x16x32_bf16 v[80:83], v[28:31], v[56:59], v[80:83]\n"
  "ds_read_b64_tr_b16 v[44:45], v4 offset:12288\n"
  "v_mfma_f32_16x16x32_bf16 v[84:87], v[28:31], v[60:63], v[84:87]\n"
  "ds_read_b64_tr_b16 v[46:47], v4 offset:12800\n"
  "s_waitcnt lgkmcnt(6)\n"
  "v_mfma_f32_16x16x32_bf16 v[92:95], v[32:35], v[60:63], v[92:95]\n"
  "ds_read_b64_tr_b16 v[28:29], v5 offset:12288\n"
  "v_mfma_f32_16x16x32_bf16 v[88:91], v[32:35], v[56:59], v[88:91]\n"
  "ds_read_b64_tr_b16 v[30:31], v5 offset:12800\n"
  "s_waitcnt lgkmcnt(6)\n"
  "v_mfma_f32_16x16x32_bf16 v[96:99], v[36:39], v[56:59], v[96:99]\n"
  "s_barrier\n"
  "ds_read_b64_tr_b16 v[32:33], v2 offset:16384\n"
  "v_mfma_f32_16x16x32_bf16 v[100:103], v[36:39], v[60:63], v[100:103]\n"
  "ds_read_b64_tr_b16 v[34:35], v2 offset:16896\n"
  "s_waitcnt lgkmcnt(6)\n"
  "v_mfma_f32_16x16x32_bf16 v[108:111], v[40:43], v[60:63], v[108:111]\n"
  "ds_read_b64_tr_b16 v[36:37], v3 offset:16384\n"
  "v_mfma_f32_16x16x32_bf16 v[104:107], v[40:43], v[56:59], v[104:107]\n"
  "ds_read_b64_tr_b16 v[38:39], v3 offset:16896\n"
  "s_waitcnt lgkmcnt(6)\n"
  "v_mfma_f32_16x16x32_bf16 v[112:115], v[44:47], v[56:59], v[112:115]\n"
  "ds_read_b64_tr_b16 v[40:41], v4 offset:16384\n"
  "v_mfma_f32_16x16x32_bf16 v[116:119], v[44:47], v[60:63], v[116:119]\n"
  "ds_read_b64_tr_b16 v[42:43], v4 offset:16896\n"
  "s_waitcnt lgkmcnt(6)\n"
  "v_mfma_f32_16x16x32_bf16 v[124:127], v[28:31], v[60:63], v[124:127]\n"
  "ds_read_b64_tr_b16 v[44:45], v5 offset:16384\n"
  "v_mfma_f32_16x16x32_bf16 v[120:123], v[28:31], v[56:59], v[120:123]\n"
  "ds_read_b64_tr_b16 v[46:47], v5 offset:16896\n"
  "s_waitcnt lgkmcnt(6)\n"
  "v_mfma_f32_16x16x32_bf16 v[128:131], v[32:35], v[48:51], 0\n"
  "ds_read_b64_tr_b16 v[28:29], v2 offset:24576\n"
  "v_mfma_f32_16x16x32_bf16 v[132:135], v[32:35], v[52:55], 0\n"
  "ds_read_b64_tr_b16 v[30:31], v2 offset:25088\n"
  "s_waitcnt lgkmcnt(6)\n"
  "v_mfma_f32_16x16x32_bf16 v[140:143], v[36:39], v[52:55], 0\n"
  "ds_read_b64_tr_b16 v[32:33], v3 offset:24576\n"
  "v_mfma_f32_16x16x32_bf16 v[136:139], v[36:39], v[48:51], 0\n"
  "ds_read_b64_tr_b16 v[34:35], v3 offset:25088\n"
  "s_waitcnt lgkmcnt(6)\n"
  "v_mfma_f32_16x16x32_bf16 v[144:147], v[40:43], v[48:51], 0\n"
  "ds_read_b64_tr_b16 v[36:37], v4 offset:24576\n"
  "v_mfma_f32_16x16x32_bf16 v[148:151], v[40:43], v[52:55], 0\n"
  "ds_read_b64_tr_b16 v[38:39], v4 offset:25088\n"
  "s_waitcnt lgkmcnt(6)\n"
  "v_mfma_f32_16x16x32_bf16 v[156:159], v[44:47], v[52:55], 0\n"
  "ds_read_b64_tr_b16 v[40:41], v5 offset:24576\n"
  "v_mfma_f32_16x16x32_bf16 v[152:155], v[44:47], v[48:51], 0\n"
  "ds_read_b64_tr_b16 v[42:43], v5 offset:25088\n"
  "s_waitcnt lgkmcnt(6)\n"
  "v_mfma_f32_16x16x32_bf16 v[160:163], v[28:31], v[48:51], 0\n"
  "ds_read_b64_tr_b16 v[44:45], v2 offset:20480\n"
  "v_mfma_f32_16x16x32_bf16 v[164:167], v[28:31], v[52:55], 0\n"
  "ds_read_b64_tr_b16 v[46:47], v2 offset:20992\n"
  "s_waitcnt lgkmcnt(6)\n"
  "v_mfma_f32_16x16x32_bf16 v[172:175], v[32:35], v[52:55], 0\n"
  "ds_read_b64_tr_b16 v[28:29], v3 offset:20480\n"
  "v_mfma_f32_16x16x32_bf16 v[168:171], v[32:35], v[48:51], 0\n"
  "ds_read_b64_tr_b16 v[30:31], v3 offset:20992\n"
  "s_waitcnt lgkmcnt(6)\n"
  "v_mfma_f32_16x16x32_bf16 v[176:179], v[36:39], v[48:51], 0\n"
  "ds_read_b64_tr_b16 v[32:33], v4 offset:20480\n"
  "v_mfma_f32_16x16x32_bf16 v[180:183], v[36:39], v[52:55], 0\n"
  "ds_read_b64_tr_b16 v[34:35], v4 offset:20992\n"
  "s_waitcnt lgkmcnt(6)\n"
  "v_mfma_f32_16x16x32_bf16 v[188:191], v[40:43], v[52:55], 0\n"
  "ds_read_b64_tr_b16 v[36:37], v5 offset:20480\n"
  "v_mfma_f32_16x16x32_bf16 v[184:187], v[40:43], v[48:51], 0\n"
  "ds_read_b64_tr_b16 v[38:39], v5 offset:20992\n"
  "s_waitcnt lgkmcnt(6)\n"
  "v_mfma_f32_16x16x32_bf16 v[128:131], v[44:47], v[56:59], v[128:131]\n"
  "ds_read_b64_tr_b16 v[40:41], v2 offset:28672\n"
  "v_mfma_f32_16x16x32_bf16 v[132:135], v[44:47], v[60:63], v[132:135]\n"
  "ds_read_b64_tr_b16 v[42:43], v2 offset:29184\n"
  "s_waitcnt lgkmcnt(6)\n"
  "v_mfma_f32_16x16x32_bf16 v[140:143], v[28:31], v[60:63], v[140:143]\n"
  "ds_read_b64_tr_b16 v[44:45], v3 offset:28672\n"
  "v_mfma_f32_16x16x32_bf16 v[136:139], v[28:31], v[56:59], v[136:139]\n"
  "ds_read_b64_tr_b16 v[46:47], v3 offset:29184\n"
  "s_waitcnt lgkmcnt(6)\n"
  "v_mfma_f32_16x16x32_bf16 v[144:147], v[32:35], v[56:59], v[144:147]\n"
  "ds_read_b64_tr_b16 v[28:29], v4 offset:28672\n"
  "v_mfma_f32_16x16x32_bf16 v[148:151], v[32:35], v[60:63], v[148:151]\n"
  "ds_read_b64_tr_b16 v[30:31], v4 offset:29184\n"
  "s_waitcnt lgkmcnt(6)\n"
  "v_mfma_f32_16x16x32_bf16 v[156:159], v[36:39], v[60:63], v[156:159]\n"
  "ds_read_b64_tr_b16 v[32:33], v5 offset:28672\n"
  "v_mfma_f32_16x16x32_bf16 v[152:155], v[36:39], v[56:59], v[152:155]\n"
  "ds_read_b64_tr_b16 v[34:35], v5 offset:29184\n"
  "s_waitcnt lgkmcnt(6)\n"
  "v_mfma_f32_16x16x32_bf16 v[160:163], v[40:43], v[56:59], v[160:163]\n"
  "s_barrier\n"
  "ds_read_b64_tr_b16 v[36:37], v2 offset:32768\n"
  "v_mfma_f32_16x16x32_bf16 v[164:167], v[40:43], v[60:63], v[164:167]\n"
  "ds_read_b64_tr_b16 v[38:39], v2 offset:33280\n"
  "ds_read_b128 v[48:51], v10 offset:16384\n"
  "ds_read_b128 v[52:55], v10 offset:18432\n"
  "s_waitcnt lgkmcnt(8)\n"
  "v_mfma_f32_16x16x32_bf16 v[172:175], v[44:47], v[60:63], v[172:175]\n"
  "ds_read_b64_tr_b16 v[40:41], v3 offset:32768\n"
  "v_mfma_f32_16x16x32_bf16 v[168:171], v[44:47], v[56:59], v[168:171]\n"
  "ds_read_b64_tr_b16 v[42:43], v3 offset:33280\n"
  "s_waitcnt lgkmcnt(8)\n"
  "v_mfma_f32_16x16x32_bf16 v[176:179], v[28:31], v[56:59], v[176:179]\n"
  "ds_read_b64_tr_b16 v[44:45], v4 offset:32768\n"
  "v_mfma_f32_16x16x32_bf16 v[180:183], v[28:31], v[60:63], v[180:183]\n"
  "ds_read_b64_tr_b16 v[46:47], v4 offset:33280\n"
  "s_waitcnt lgkmcnt(8)\n"
  "v_mfma_f32_16x16x32_bf16 v[188:191], v[32:35], v[60:63], v[188:191]\n"
  "ds_read_b64_tr_b16 v[28:29], v5 offset:32768\n"
  "v_mfma_f32_16x16x32_bf16 v[184:187], v[32:35], v[56:59], v[184:187]\n"
  "ds_read_b64_tr_b16 v[30:31], v5 offset:33280\n"
  "s_waitcnt lgkmcnt(7)\n"
  "v_mfma_f32_16x16x32_bf16 v[64:67], v[36:39], v[48:51], v[64:67]\n"
  "ds_read_b64_tr_b16 v[32:33], v2 offset:40960\n"
  "s_waitcnt lgkmcnt(7)\n"
  "v_mfma_f32_16x16x32_bf16 v[68:71], v[36:39], v[52:55], v[68:71]\n"
  "ds_read_b64_tr_b16 v[34:35], v2 offset:41472\n"
  "ds_read_b128 v[56:59], v11 offset:16384\n"
  "ds_read_b128 v[60:63], v11 offset:18432\n"
  "s_waitcnt lgkmcnt(8)\n"
  "v_mfma_f32_16x16x32_bf16 v[76:79], v[40:43], v[52:55], v[76:79]\n"
  "ds_read_b64_tr_b16 v[36:37], v3 offset:40960\n"
  "v_mfma_f32_16x16x32_bf16 v[72:75], v[40:43], v[48:51], v[72:75]\n"
  "ds_read_b64_tr_b16 v[38:39], v3 offset:41472\n"
  "s_waitcnt lgkmcnt(8)\n"
  "v_mfma_f32_16x16x32_bf16 v[80:83], v[44:47], v[48:51], v[80:83]\n"
  "ds_read_b64_tr_b16 v[40:41], v4 offset:40960\n"
  "v_mfma_f32_16x16x32_bf16 v[84:87], v[44:47], v[52:55], v[84:87]\n"
  "ds_read_b64_tr_b16 v[42:43], v4 offset:41472\n"
  "s_waitcnt lgkmcnt(8)\n"
  "v_mfma_f32_16x16x32_bf16 v[92:95], v[28:31], v[52:55], v[92:95]\n"
  "ds_read_b64_tr_b16 v[44:45], v5 offset:40960\n"
  "v_mfma_f32_16x16x32_bf16 v[88:91], v[28:31], v[48:51], v[88:91]\n"
  "ds_read_b64_tr_b16 v[46:47], v5 offset:41472\n"
  "s_waitcnt lgkmcnt(8)\n"
  "v_mfma_f32_16x16x32_bf16 v[96:99], v[32:35], v[48:51], v[96:99]\n"
  "ds_read_b64_tr_b16 v[28:29], v2 offset:36864\n"
  "v_mfma_f32_16x16x32_bf16 v[100:103], v[32:35], v[52:55], v[100:103]\n"
  "ds_read_b64_tr_b16 v[30:31], v2 offset:37376\n"
  "s_waitcnt lgkmcnt(6)\n"
  "v_mfma_f32_16x16x32_bf16 v[108:111], v[36:39], v[52:55], v[108:111]\n"
  "ds_read_b64_tr_b16 v[32:33], v3 offset:36864\n"
  "v_mfma_f32_16x16x32_bf16 v[104:107], v[36:39], v[48:51], v[104:107]\n"
  "ds_read_b64_tr_b16 v[34:35], v3 offset:37376\n"
  "s_waitcnt lgkmcnt(6)\n"
  "v_mfma_f32_16x16x32_bf16 v[112:115], v[40:43], v[48:51], v[112:115]\n"
  "ds_read_b64_tr_b16 v[36:37], v4 offset:36864\n"
  "v_mfma_f32_16x16x32_bf16 v[116:119], v[40:43], v[52:55], v[116:119]\n"
  "ds_read_b64_tr_b16 v[38:39], v4 offset:37376\n"
  "s_waitcnt lgkmcnt(6)\n"
  "v_mfma_f32_16x16x32_bf16 v[124:127], v[44:47], v[52:55], v[124:127]\n"
  "ds_read_b64_tr_b16 v[40:41], v5 offset:36864\n"
  "v_mfma_f32_16x16x32_bf16 v[120:123], v[44:47], v[48:51], v[120:123]\n"
  "ds_read_b64_tr_b16 v[42:43], v5 offset:37376\n"
  "s_waitcnt lgkmcnt(6)\n"
  "v_mfma_f32_16x16x32_bf16 v[64:67], v[28:31], v[56:59], v[64:67]\n"
  "ds_read_b64_tr_b16 v[44:45], v2 offset:45056\n"
  "v_mfma_f32_16x16x32_bf16 v[68:71], v[28:31], v[60:63], v[68:71]\n"
  "ds_read_b64_tr_b16 v[46:47], v2 offset:45568\n"
  "s_waitcnt lgkmcnt(6)\n"
  "v_mfma_f32_16x16x32_bf16 v[76:79], v[32:35], v[60:63], v[76:79]\n"
  "ds_read_b64_tr_b16 v[28:29], v3 offset:45056\n"
  "v_mfma_f32_16x16x32_bf16 v[72:75], v[32:35], v[56:59], v[72:75]\n"
  "ds_read_b64_tr_b16 v[30:31], v3 offset:45568\n"
  "s_waitcnt lgkmcnt(6)\n"
  "v_mfma_f32_16x16x32_bf16 v[80:83], v[36:39], v[56:59], v[80:83]\n"
  "ds_read_b64_tr_b16 v[32:33], v4 offset:45056\n"
  "v_mfma_f32_16x16x32_bf16 v[84:87], v[36:39], v[60:63], v[84:87]\n"
  "ds_read_b64_tr_b16 v[34:35], v4 offset:45568\n"
  "s_waitcnt lgkmcnt(6)\n"
  "v_mfma_f32_16x16x32_bf16 v[92:95], v[40:43], v[60:63], v[92:95]\n"
  "ds_read_b64_tr_b16 v[36:37], v5 offset:45056\n"
  "v_mfma_f32_16x16x32_bf16 v[88:91], v[40:43], v[56:59], v[88:91]\n"
  "ds_read_b64_tr_b16 v[38:39], v5 offset:45568\n"
  "s_waitcnt lgkmcnt(6)\n"
  "v_mfma_f32_16x16x32_bf16 v[96:99], v[44:47], v[56:59], v[96:99]\n"
  "s_barrier\n"
  "ds_read_b64_tr_b16 v[40:41], v2 offset:49152\n"
  "v_mfma_f32_16x16x32_bf16 v[100:103], v[44:47], v[60:63], v[100:103]\n"
  "ds_read_b64_tr_b16 v[42:43], v2 offset:49664\n"
  "s_waitcnt lgkmcnt(6)\n"
  "v_mfma_f32_16x16x32_bf16 v[108:111], v[28:31], v[60:63], v[108:111]\n"
  "ds_read_b64_tr_b16 v[44:45], v3 offset:49152\n"
  "v_mfma_f32_16x16x32_bf16 v[104:107], v[28:31], v[56:59], v[104:107]\n"
  "ds_read_b64_tr_b16 v[46:47], v3 offset:49664\n"
  "s_waitcnt lgkmcnt(6)\n"
  "v_mfma_f32_16x16x32_bf16 v[112:115], v[32:35], v[56:59], v[112:115]\n"
  "ds_read_b64_tr_b16 v[28:29], v4 offset:49152\n"
  "v_mfma_f32_16x16x32_bf16 v[116:119], v[32:35], v[60:63], v[116:119]\n"
  "ds_read_b64_tr_b16 v[30:31], v4 offset:49664\n"
  "s_waitcnt lgkmcnt(6)\n"
  "v_mfma_f32_16x16x32_bf16 v[124:127], v[36:39], v[60:63], v[124:127]\n"
  "ds_read_b64_tr_b16 v[32:33], v5 offset:49152\n"
  "v_mfma_f32_16x16x32_bf16 v[120:123], v[36:39], v[56:59], v[120:123]\n"
  "ds_read_b64_tr_b16 v[34:35], v5 offset:49664\n"
  "s_waitcnt lgkmcnt(6)\n"
  "v_mfma_f32_16x16x32_bf16 v[128:131], v[40:43], v[48:51], v[128:131]\n"
  "ds_read_b64_tr_b16 v[36:37], v2 offset:57344\n"
  "v_mfma_f32_16x16x32_bf16 v[132:135], v[40:43], v[52:55], v[132:135]\n"
  "ds_read_b64_tr_b16 v[38:39], v2 offset:57856\n"
  "s_waitcnt lgkmcnt(6)\n"
  "v_mfma_f32_16x16x32_bf16 v[140:143], v[44:47], v[52:55], v[140:143]\n"
  "ds_read_b64_tr_b16 v[40:41], v3 offset:57344\n"
  "v_mfma_f32_16x16x32_bf16 v[136:139], v[44:47], v[48:51], v[136:139]\n"
  "ds_read_b64_tr_b16 v[42:43], v3 offset:57856\n"
  "s_waitcnt lgkmcnt(6)\n"
  "v_mfma_f32_16x16x32_bf16 v[144:147], v[28:31], v[48:51], v[144:147]\n"
  "ds_read_b64_tr_b16 v[44:45], v4 offset:57344\n"
  "v_mfma_f32_16x16x32_bf16 v[148:151], v[28:31], v[52:55], v[148:151]\n"
  "ds_read_b64_tr_b16 v[46:47], v4 offset:57856\n"
  "s_waitcnt lgkmcnt(6)\n"
  "v_mfma_f32_16x16x32_bf16 v[156:159], v[32:35], v[52:55], v[156:159]\n"
  "ds_read_b64_tr_b16 v[28:29], v5 offset:57344\n"
  "v_mfma_f32_16x16x32_bf16 v[152:155], v[32:35], v[48:51], v[152:155]\n"
  "ds_read_b64_tr_b16 v[30:31], v5 offset:57856\n"
  "s_waitcnt lgkmcnt(6)\n"
  "v_mfma_f32_16x16x32_bf16 v[160:163], v[36:39], v[48:51], v[160:163]\n"
  "ds_read_b64_tr_b16 v[32:33], v2 offset:53248\n"
  "v_mfma_f32_16x16x32_bf16 v[164:167], v[36:39], v[52:55], v[164:167]\n"
  "ds_read_b64_tr_b16 v[34:35], v2 offset:53760\n"
  "s_waitcnt lgkmcnt(6)\n"
  "v_mfma_f32_16x16x32_bf16 v[172:175], v[40:43], v[52:55], v[172:175]\n"
  "ds_read_b64_tr_b16 v[36:37], v3 offset:53248\n"
  "v_mfma_f32_16x16x32_bf16 v[168:171], v[40:43], v[48:51], v[168:171]\n"
  "ds_read_b64_tr_b16 v[38:39], v3 offset:53760\n"
  "s_waitcnt lgkmcnt(6)\n"
  "v_mfma_f32_16x16x32_bf16 v[176:179], v[44:47], v[48:51], v[176:179]\n"
  "ds_read_b64_tr_b16 v[40:41], v4 offset:53248\n"
  "v_mfma_f32_16x16x32_bf16 v[180:183], v[44:47], v[52:55], v[180:183]\n"
  "ds_read_b64_tr_b16 v[42:43], v4 offset:53760\n"
  "s_waitcnt lgkmcnt(6)\n"
  "v_mfma_f32_16x16x32_bf16 v[188:191], v[28:31], v[52:55], v[188:191]\n"
  "ds_read_b64_tr_b16 v[44:45], v5 offset:53248\n"
  "v_mfma_f32_16x16x32_bf16 v[184:187], v[28:31], v[48:51], v[184:187]\n"
  "ds_read_b64_tr_b16 v[46:47], v5 offset:53760\n"
  "s_waitcnt lgkmcnt(6)\n"
  "v_mfma_f32_16x16x32_bf16 v[128:131], v[32:35], v[56:59], v[128:131]\n"
  "ds_read_b64_tr_b16 v[28:29], v2 offset:61440\n"
  "v_mfma_f32_16x16x32_bf16 v[132:135], v[32:35], v[60:63], v[132:135]\n"
  "ds_read_b64_tr_b16 v[30:31], v2 offset:61952\n"
  "s_waitcnt lgkmcnt(6)\n"
  "v_mfma_f32_16x16x32_bf16 v[140:143], v[36:39], v[60:63], v[140:143]\n"
  "ds_read_b64_tr_b16 v[32:33], v3 offset:61440\n"
  "v_mfma_f32_16x16x32_bf16 v[136:139], v[36:39], v[56:59], v[136:139]\n"
  "ds_read_b64_tr_b16 v[34:35], v3 offset:61952\n"
  "s_waitcnt lgkmcnt(6)\n"
  "v_mfma_f32_16x16x32_bf16 v[144:147], v[40:43], v[56:59], v[144:147]\n"
  "ds_read_b64_tr_b16 v[36:37], v4 offset:61440\n"
  "v_mfma_f32_16x16x32_bf16 v[148:151], v[40:43], v[60:63], v[148:151]\n"
  "ds_read_b64_tr_b16 v[38:39], v4 offset:61952\n"
  "s_waitcnt lgkmcnt(6)\n"
  "v_mfma_f32_16x16x32_bf16 v[156:159], v[44:47], v[60:63], v[156:159]\n"
  "ds_read_b64_tr_b16 v[40:41], v5 offset:61440\n"
  "v_mfma_f32_16x16x32_bf16 v[152:155], v[44:47], v[56:59], v[152:155]\n"
  "ds_read_b64_tr_b16 v[42:43], v5 offset:61952\n"
  "s_waitcnt lgkmcnt(6)\n"
  "v_mfma_f32_16x16x32_bf16 v[160:163], v[28:31], v[56:59], v[160:163]\n"
  "s_barrier\n"
  "ds_read_b64_tr_b16 v[44:45], v2 offset:0\n"
  "v_mfma_f32_16x16x32_bf16 v[164:167], v[28:31], v[60:63], v[164:167]\n"
  "ds_read_b64_tr_b16 v[46:47], v2 offset:512\n"
  "ds_read_b128 v[48:51], v10 offset:0\n"
  "ds_read_b128 v[52:55], v10 offset:2048\n"
  "s_waitcnt lgkmcnt(8)\n"
  "v_mfma_f32_16x16x32_bf16 v[172:175], v[32:35], v[60:63], v[172:175]\n"
  "ds_read_b64_tr_b16 v[28:29], v3 offset:0\n"
  "v_mfma_f32_16x16x32_bf16 v[168:171], v[32:35], v[56:59], v[168:171]\n"
  "ds_read_b64_tr_b16 v[30:31], v3 offset:512\n"
  "s_waitcnt lgkmcnt(8)\n"
  "v_mfma_f32_16x16x32_bf16 v[176:179], v[36:39], v[56:59], v[176:179]\n"
  "ds_read_b64_tr_b16 v[32:33], v4 offset:0\n"
  "v_mfma_f32_16x16x32_bf16 v[180:183], v[36:39], v[60:63], v[180:183]\n"
  "ds_read_b64_tr_b16 v[34:35], v4 offset:512\n"
  "s_waitcnt lgkmcnt(8)\n"
  "v_mfma_f32_16x16x32_bf16 v[188:191], v[40:43], v[60:63], v[188:191]\n"
  "ds_read_b64_tr_b16 v[36:37], v5 offset:0\n"
  "v_mfma_f32_16x16x32_bf16 v[184:187], v[40:43], v[56:59], v[184:187]\n"
  "ds_read_b64_tr_b16 v[38:39], v5 offset:512\n"
  "s_waitcnt lgkmcnt(7)\n"
  "v_mfma_f32_16x16x32_bf16 v[64:67], v[44:47], v[48:51], v[64:67]\n"
  "ds_read_b64_tr_b16 v[40:41], v2 offset:8192\n"
  "s_waitcnt lgkmcnt(7)\n"
  "v_mfma_f32_16x16x32_bf16 v[68:71], v[44:47], v[52:55], v[68:71]\n"
  "ds_read_b64_tr_b16 v[42:43], v2 offset:8704\n"
  "ds_read_b128 v[56:59], v11 offset:0\n"
  "ds_read_b128 v[60:63], v11 offset:2048\n"
  "s_waitcnt lgkmcnt(8)\n"
  "v_mfma_f32_16x16x32_bf16 v[76:79], v[28:31], v[52:55], v[76:79]\n"
  "ds_read_b64_tr_b16 v[44:45], v3 offset:8192\n"
  "v_mfma_f32_16x16x32_bf16 v[72:75], v[28:31], v[48:51], v[72:75]\n"
  "ds_read_b64_tr_b16 v[46:47], v3 offset:8704\n"
  "s_waitcnt lgkmcnt(8)\n"
  "v_mfma_f32_16x16x32_bf16 v[80:83], v[32:35], v[48:51], v[80:83]\n"
  "ds_read_b64_tr_b16 v[28:29], v4 offset:8192\n"
  "v_mfma_f32_16x16x32_bf16 v[84:87], v[32:35], v[52:55], v[84:87]\n"
  "ds_read_b64_tr_b16 v[30:31], v4 offset:8704\n"
  "s_waitcnt lgkmcnt(8)\n"
  "v_mfma_f32_16x16x32_bf16 v[92:95], v[36:39], v[52:55], v[92:95]\n"
  "ds_read_b64_tr_b16 v[32:33], v5 offset:8192\n"
  "v_mfma_f32_16x16x32_bf16 v[88:91], v[36:39], v[48:51], v[88:91]\n"
  "ds_read_b64_tr_b16 v[34:35], v5 offset:8704\n"
  "s_waitcnt lgkmcnt(8)\n"
  "v_mfma_f32_16x16x32_bf16 v[96:99], v[40:43], v[48:51], v[96:99]\n"
  "ds_read_b64_tr_b16 v[36:37], v2 offset:4096\n"
  "v_mfma_f32_16x16x32_bf16 v[100:103], v[40:43], v[52:55], v[100:103]\n"
  "ds_read_b64_tr_b16 v[38:39], v2 offset:4608\n"
  "s_waitcnt lgkmcnt(6)\n"
  "v_mfma_f32_16x16x32_bf16 v[108:111], v[44:47], v[52:55], v[108:111]\n"
  "ds_read_b64_tr_b16 v[40:41], v3 offset:4096\n"
  "v_mfma_f32_16x16x32_bf16 v[104:107], v[44:47], v[48:51], v[104:107]\n"
  "ds_read_b64_tr_b16 v[42:43], v3 offset:4608\n"
  "s_waitcnt lgkmcnt(6)\n"
  "v_mfma_f32_16x16x32_bf16 v[112:115], v[28:31], v[48:51], v[112:115]\n"
  "ds_read_b64_tr_b16 v[44:45], v4 offset:4096\n"
  "v_mfma_f32_16x16x32_bf16 v[116:119], v[28:31], v[52:55], v[116:119]\n"
  "ds_read_b64_tr_b16 v[46:47], v4 offset:4608\n"
  "s_waitcnt lgkmcnt(6)\n"
  "v_mfma_f32_16x16x32_bf16 v[124:127], v[32:35], v[52:55], v[124:127]\n"
  "ds_read_b64_tr_b16 v[28:29], v5 offset:4096\n"
  "v_mfma_f32_16x16x32_bf16 v[120:123], v[32:35], v[48:51], v[120:123]\n"
  "ds_read_b64_tr_b16 v[30:31], v5 offset:4608\n"
  "s_waitcnt lgkmcnt(6)\n"
  "v_mfma_f32_16x16x32_bf16 v[64:67], v[36:39], v[56:59], v[64:67]\n"
  "ds_read_b64_tr_b16 v[32:33], v2 offset:12288\n"
  "v_mfma_f32_16x16x32_bf16 v[68:71], v[36:39], v[60:63], v[68:71]\n"
  "ds_read_b64_tr_b16 v[34:35], v2 offset:12800\n"
  "s_waitcnt lgkmcnt(6)\n"
  "v_mfma_f32_16x16x32_bf16 v[76:79], v[40:43], v[60:63], v[76:79]\n"
  "ds_read_b64_tr_b16 v[36:37], v3 offset:12288\n"
  "v_mfma_f32_16x16x32_bf16 v[72:75], v[40:43], v[56:59], v[72:75]\n"
  "ds_read_b64_tr_b16 v[38:39], v3 offset:12800\n"
  "s_waitcnt lgkmcnt(6)\n"
  "v_mfma_f32_16x16x32_bf16 v[80:83], v[44:47], v[56:59], v[80:83]\n"
  "ds_read_b64_tr_b16 v[40:41], v4 offset:12288\n"
  "v_mfma_f32_16x16x32_bf16 v[84:87], v[44:47], v[60:63], v[84:87]\n"
  "ds_read_b64_tr_b16 v[42:43], v4 offset:12800\n"
  "s_waitcnt lgkmcnt(6)\n"
  "v_mfma_f32_16x16x32_bf16 v[92:95], v[28:31], v[60:63], v[92:95]\n"
  "ds_read_b64_tr_b16 v[44:45], v5 offset:12288\n"
  "v_mfma_f32_16x16x32_bf16 v[88:91], v[28:31], v[56:59], v[88:91]\n"
  "ds_read_b64_tr_b16 v[46:47], v5 offset:12800\n"
  "s_waitcnt lgkmcnt(6)\n"
  "v_mfma_f32_16x16x32_bf16 v[96:99], v[32:35], v[56:59], v[96:99]\n"
  "s_barrier\n"
  "ds_read_b64_tr_b16 v[28:29], v2 offset:16384\n"
  "v_mfma_f32_16x16x32_bf16 v[100:103], v[32:35], v[60:63], v[100:103]\n"
  "ds_read_b64_tr_b16 v[30:31], v2 offset:16896\n"
  "s_waitcnt lgkmcnt(6)\n"
  "v_mfma_f32_16x16x32_bf16 v[108:111], v[36:39], v[60:63], v[108:111]\n"
  "ds_read_b64_tr_b16 v[32:33], v3 offset:16384\n"
  "v_mfma_f32_16x16x32_bf16 v[104:107], v[36:39], v[56:59], v[104:107]\n"
  "ds_read_b64_tr_b16 v[34:35], v3 offset:16896\n"
  "s_waitcnt lgkmcnt(6)\n"
  "v_mfma_f32_16x16x32_bf16 v[112:115], v[40:43], v[56:59], v[112:115]\n"
  "ds_read_b64_tr_b16 v[36:37], v4 offset:16384\n"
  "v_mfma_f32_16x16x32_bf16 v[116:119], v[40:43], v[60:63], v[116:119]\n"
  "ds_read_b64_tr_b16 v[38:39], v4 offset:16896\n"
  "s_waitcnt lgkmcnt(6)\n"
  "v_mfma_f32_16x16x32_bf16 v[124:127], v[44:47], v[60:63], v[124:127]\n"
  "ds_read_b64_tr_b16 v[40:41], v5 offset:16384\n"
  "v_mfma_f32_16x16x32_bf16 v[120:123], v[44:47], v[56:59], v[120:123]\n"
  "ds_read_b64_tr_b16 v[42:43], v5 offset:16896\n"
  "s_waitcnt lgkmcnt(6)\n"
  "v_mfma_f32_16x16x32_bf16 v[128:131], v[28:31], v[48:51], v[128:131]\n"
  "ds_read_b64_tr_b16 v[44:45], v2 offset:24576\n"
  "v_mfma_f32_16x16x32_bf16 v[132:135], v[28:31], v[52:55], v[132:135]\n"
  "ds_read_b64_tr_b16 v[46:47], v2 offset:25088\n"
  "s_waitcnt lgkmcnt(6)\n"
  "v_mfma_f32_16x16x32_bf16 v[140:143], v[32:35], v[52:55], v[140:143]\n"
  "ds_read_b64_tr_b16 v[28:29], v3 offset:24576\n"
  "v_mfma_f32_16x16x32_bf16 v[136:139], v[32:35], v[48:51], v[136:139]\n"
  "ds_read_b64_tr_b16 v[30:31], v3 offset:25088\n"
  "s_waitcnt lgkmcnt(6)\n"
  "v_mfma_f32_16x16x32_bf16 v[144:147], v[36:39], v[48:51], v[144:147]\n"
  "ds_read_b64_tr_b16 v[32:33], v4 offset:24576\n"
  "v_mfma_f32_16x16x32_bf16 v[148:151], v[36:39], v[52:55], v[148:151]\n"
  "ds_read_b64_tr_b16 v[34:35], v4 offset:25088\n"
  "s_waitcnt lgkmcnt(6)\n"
  "v_mfma_f32_16x16x32_bf16 v[156:159], v[40:43], v[52:55], v[156:159]\n"
  "ds_read_b64_tr_b16 v[36:37], v5 offset:24576\n"
  "v_mfma_f32_16x16x32_bf16 v[152:155], v[40:43], v[48:51], v[152:155]\n"
  "ds_read_b64_tr_b16 v[38:39], v5 offset:25088\n"
  "s_waitcnt lgkmcnt(6)\n"
  "v_mfma_f32_16x16x32_bf16 v[160:163], v[44:47], v[48:51], v[160:163]\n"
  "ds_read_b64_tr_b16 v[40:41], v2 offset:20480\n"
  "v_mfma_f32_16x16x32_bf16 v[164:167], v[44:47], v[52:55], v[164:167]\n"
  "ds_read_b64_tr_b16 v[42:43], v2 offset:20992\n"
  "s_waitcnt lgkmcnt(6)\n"
  "v_mfma_f32_16x16x32_bf16 v[172:175], v[28:31], v[52:55], v[172:175]\n"
  "ds_read_b64_tr_b16 v[44:45], v3 offset:20480\n"
  "v_mfma_f32_16x16x32_bf16 v[168:171], v[28:31], v[48:51], v[168:171]\n"
  "ds_read_b64_tr_b16 v[46:47], v3 offset:20992\n"
  "s_waitcnt lgkmcnt(6)\n"
  "v_mfma_f32_16x16x32_bf16 v[176:179], v[32:35], v[48:51], v[176:179]\n"
  "ds_read_b64_tr_b16 v[28:29], v4 offset:20480\n"
  "v_mfma_f32_16x16x32_bf16 v[180:183], v[32:35], v[52:55], v[180:183]\n"
  "ds_read_b64_tr_b16 v[30:31], v4 offset:20992\n"
  "s_waitcnt lgkmcnt(6)\n"
  "v_mfma_f32_16x16x32_bf16 v[188:191], v[36:39], v[52:55], v[188:191]\n"
  "ds_read_b64_tr_b16 v[32:33], v5 offset:20480\n"
  "v_mfma_f32_16x16x32_bf16 v[184:187], v[36:39], v[48:51], v[184:187]\n"
  "ds_read_b64_tr_b16 v[34:35], v5 offset:20992\n"
  "s_waitcnt lgkmcnt(6)\n"
  "v_mfma_f32_16x16x32_bf16 v[128:131], v[40:43], v[56:59], v[128:131]\n"
  "ds_read_b64_tr_b16 v[36:37], v2 offset:28672\n"
  "v_mfma_f32_16x16x32_bf16 v[132:135], v[40:43], v[60:63], v[132:135]\n"
  "ds_read_b64_tr_b16 v[38:39], v2 offset:29184\n"
  "s_waitcnt lgkmcnt(6)\n"
  "v_mfma_f32_16x16x32_bf16 v[140:143], v[44:47], v[60:63], v[140:143]\n"
  "ds_read_b64_tr_b16 v[40:41], v3 offset:28672\n"
  "v_mfma_f32_16x16x32_bf16 v[136:139], v[44:47], v[56:59], v[136:139]\n"
  "ds_read_b64_tr_b16 v[42:43], v3 offset:29184\n"
  "s_waitcnt lgkmcnt(6)\n"
  "v_mfma_f32_16x16x32_bf16 v[144:147], v[28:31], v[56:59], v[144:147]\n"
  "ds_read_b64_tr_b16 v[44:45], v4 offset:28672\n"
  "v_mfma_f32_16x16x32_bf16 v[148:151], v[28:31], v[60:63], v[148:151]\n"
  "ds_read_b64_tr_b16 v[46:47], v4 offset:29184\n"
  "s_waitcnt lgkmcnt(6)\n"
  "v_mfma_f32_16x16x32_bf16 v[156:159], v[32:35], v[60:63], v[156:159]\n"
  "ds_read_b64_tr_b16 v[28:29], v5 offset:28672\n"
  "v_mfma_f32_16x16x32_bf16 v[152:155], v[32:35], v[56:59], v[152:155]\n"
  "ds_read_b64_tr_b16 v[30:31], v5 offset:29184\n"
  "s_waitcnt lgkmcnt(6)\n"
  "v_mfma_f32_16x16x32_bf16 v[160:163], v[36:39], v[56:59], v[160:163]\n"
  "s_barrier\n"
  "ds_read_b64_tr_b16 v[32:33], v2 offset:32768\n"
  "v_mfma_f32_16x16x32_bf16 v[164:167], v[36:39], v[60:63], v[164:167]\n"
  "ds_read_b64_tr_b16 v[34:35], v2 offset:33280\n"
  "ds_read_b128 v[48:51], v10 offset:16384\n"
  "ds_read_b128 v[52:55], v10 offset:18432\n"
  "s_waitcnt lgkmcnt(8)\n"
  "v_mfma_f32_16x16x32_bf16 v[172:175], v[40:43], v[60:63], v[172:175]\n"
  "ds_read_b64_tr_b16 v[36:37], v3 offset:32768\n"
  "v_mfma_f32_16x16x32_bf16 v[168:171], v[40:43], v[56:59], v[168:171]\n"
  "ds_read_b64_tr_b16 v[38:39], v3 offset:33280\n"
  "s_waitcnt lgkmcnt(8)\n"
  "v_mfma_f32_16x16x32_bf16 v[176:179], v[44:47], v[56:59], v[176:179]\n"
  "ds_read_b64_tr_b16 v[40:41], v4 offset:32768\n"
  "v_mfma_f32_16x16x32_bf16 v[180:183], v[44:47], v[60:63], v[180:183]\n"
  "ds_read_b64_tr_b16 v[42:43], v4 offset:33280\n"
  "s_waitcnt lgkmcnt(8)\n"
  "v_mfma_f32_16x16x32_bf16 v[188:191], v[28:31], v[60:63], v[188:191]\n"
  "ds_read_b64_tr_b16 v[44:45], v5 offset:32768\n"
  "v_mfma_f32_16x16x32_bf16 v[184:187], v[28:31], v[56:59], v[184:187]\n"
  "ds_read_b64_tr_b16 v[46:47], v5 offset:33280\n"
  "s_waitcnt lgkmcnt(7)\n"
  "v_mfma_f32_16x16x32_bf16 v[64:67], v[32:35], v[48:51], v[64:67]\n"
  "ds_read_b64_tr_b16 v[28:29], v2 offset:40960\n"
  "s_waitcnt lgkmcnt(7)\n"
  "v_mfma_f32_16x16x32_bf16 v[68:71], v[32:35], v[52:55], v[68:71]\n"
  "ds_read_b64_tr_b16 v[30:31], v2 offset:41472\n"
  "ds_read_b128 v[56:59], v11 offset:16384\n"
  "ds_read_b128 v[60:63], v11 offset:18432\n"
  "s_waitcnt lgkmcnt(8)\n"
  "v_mfma_f32_16x16x32_bf16 v[76:79], v[36:39], v[52:55], v[76:79]\n"
  "ds_read_b64_tr_b16 v[32:33], v3 offset:40960\n"
  "v_mfma_f32_16x16x32_bf16 v[72:75], v[36:39], v[48:51], v[72:75]\n"
  "ds_read_b64_tr_b16 v[34:35], v3 offset:41472\n"
  "s_waitcnt lgkmcnt(8)\n"
  "v_mfma_f32_16x16x32_bf16 v[80:83], v[40:43], v[48:51], v[80:83]\n"
  "ds_read_b64_tr_b16 v[36:37], v4 offset:40960\n"
  "v_mfma_f32_16x16x32_bf16 v[84:87], v[40:43], v[52:55], v[84:87]\n"
  "ds_read_b64_tr_b16 v[38:39], v4 offset:41472\n"
  "s_waitcnt lgkmcnt(8)\n"
  "v_mfma_f32_16x16x32_bf16 v[92:95], v[44:47], v[52:55], v[92:95]\n"
  "ds_read_b64_tr_b16 v[40:41], v5 offset:40960\n"
  "v_mfma_f32_16x16x32_bf16 v[88:91], v[44:47], v[48:51], v[88:91]\n"
  "ds_read_b64_tr_b16 v[42:43], v5 offset:41472\n"
  "s_waitcnt lgkmcnt(8)\n"
  "v_mfma_f32_16x16x32_bf16 v[96:99], v[28:31], v[48:51], v[96:99]\n"
  "ds_read_b64_tr_b16 v[44:45], v2 offset:36864\n"
  "v_mfma_f32_16x16x32_bf16 v[100:103], v[28:31], v[52:55], v[100:103]\n"
  "ds_read_b64_tr_b16 v[46:47], v2 offset:37376\n"
  "s_waitcnt lgkmcnt(6)\n"
  "v_mfma_f32_16x16x32_bf16 v[108:111], v[32:35], v[52:55], v[108:111]\n"
  "ds_read_b64_tr_b16 v[28:29], v3 offset:36864\n"
  "v_mfma_f32_16x16x32_bf16 v[104:107], v[32:35], v[48:51], v[104:107]\n"
  "ds_read_b64_tr_b16 v[30:31], v3 offset:37376\n"
  "s_waitcnt lgkmcnt(6)\n"
  "v_mfma_f32_16x16x32_bf16 v[112:115], v[36:39], v[48:51], v[112:115]\n"
  "ds_read_b64_tr_b16 v[32:33], v4 offset:36864\n"
  "v_mfma_f32_16x16x32_bf16 v[116:119], v[36:39], v[52:55], v[116:119]\n"
  "ds_read_b64_tr_b16 v[34:35], v4 offset:37376\n"
  "s_waitcnt lgkmcnt(6)\n"
  "v_mfma_f32_16x16x32_bf16 v[124:127], v[40:43], v[52:55], v[124:127]\n"
  "ds_read_b64_tr_b16 v[36:37], v5 offset:36864\n"
  "v_mfma_f32_16x16x32_bf16 v[120:123], v[40:43], v[48:51], v[120:123]\n"
  "ds_read_b64_tr_b16 v[38:39], v5 offset:37376\n"
  "s_waitcnt lgkmcnt(6)\n"
  "v_mfma_f32_16x16x32_bf16 v[64:67], v[44:47], v[56:59], v[64:67]\n"
  "ds_read_b64_tr_b16 v[40:41], v2 offset:45056\n"
  "v_mfma_f32_16x16x32_bf16 v[68:71], v[44:47], v[60:63], v[68:71]\n"
  "ds_read_b64_tr_b16 v[42:43], v2 offset:45568\n"
  "s_waitcnt lgkmcnt(6)\n"
  "v_mfma_f32_16x16x32_bf16 v[76:79], v[28:31], v[60:63], v[76:79]\n"
  "ds_read_b64_tr_b16 v[44:45], v3 offset:45056\n"
  "v_mfma_f32_16x16x32_bf16 v[72:75], v[28:31], v[56:59], v[72:75]\n"
  "ds_read_b64_tr_b16 v[46:47], v3 offset:45568\n"
  "s_waitcnt lgkmcnt(6)\n"
  "v_mfma_f32_16x16x32_bf16 v[80:83], v[32:35], v[56:59], v[80:83]\n"
  "ds_read_b64_tr_b16 v[28:29], v4 offset:45056\n"
  "v_mfma_f32_16x16x32_bf16 v[84:87], v[32:35], v[60:63], v[84:87]\n"
  "ds_read_b64_tr_b16 v[30:31], v4 offset:45568\n"
  "s_waitcnt lgkmcnt(6)\n"
  "v_mfma_f32_16x16x32_bf16 v[92:95], v[36:39], v[60:63], v[92:95]\n"
  "ds_read_b64_tr_b16 v[32:33], v5 offset:45056\n"
  "v_mfma_f32_16x16x32_bf16 v[88:91], v[36:39], v[56:59], v[88:91]\n"
  "ds_read_b64_tr_b16 v[34:35], v5 offset:45568\n"
  "s_waitcnt lgkmcnt(6)\n"
  "v_mfma_f32_16x16x32_bf16 v[96:99], v[40:43], v[56:59], v[96:99]\n"
  "s_barrier\n"
  "ds_read_b64_tr_b16 v[36:37], v2 offset:49152\n"
  "v_mfma_f32_16x16x32_bf16 v[100:103], v[40:43], v[60:63], v[100:103]\n"
  "ds_read_b64_tr_b16 v[38:39], v2 offset:49664\n"
  "s_waitcnt lgkmcnt(6)\n"
  "v_mfma_f32_16x16x32_bf16 v[108:111], v[44:47], v[60:63], v[108:111]\n"
  "ds_read_b64_tr_b16 v[40:41], v3 offset:49152\n"
  "v_mfma_f32_16x16x32_bf16 v[104:107], v[44:47], v[56:59], v[104:107]\n"
  "ds_read_b64_tr_b16 v[42:43], v3 offset:49664\n"
  "s_waitcnt lgkmcnt(6)\n"
  "v_mfma_f32_16x16x32_bf16 v[112:115], v[28:31], v[56:59], v[112:115]\n"
  "ds_read_b64_tr_b16 v[44:45], v4 offset:49152\n"
  "v_mfma_f32_16x16x32_bf16 v[116:119], v[28:31], v[60:63], v[116:119]\n"
  "ds_read_b64_tr_b16 v[46:47], v4 offset:49664\n"
  "s_waitcnt lgkmcnt(6)\n"
  "v_mfma_f32_16x16x32_bf16 v[124:127], v[32:35], v[60:63], v[124:127]\n"
  "ds_read_b64_tr_b16 v[28:29], v5 offset:49152\n"
  "v_mfma_f32_16x16x32_bf16 v[120:123], v[32:35], v[56:59], v[120:123]\n"
  "ds_read_b64_tr_b16 v[30:31], v5 offset:49664\n"
  "s_waitcnt lgkmcnt(6)\n"
  "v_mfma_f32_16x16x32_bf16 v[128:131], v[36:39], v[48:51], v[128:131]\n"
  "ds_read_b64_tr_b16 v[32:33], v2 offset:57344\n"
  "v_mfma_f32_16x16x32_bf16 v[132:135], v[36:39], v[52:55], v[132:135]\n"
  "ds_read_b64_tr_b16 v[34:35], v2 offset:57856\n"
  "s_waitcnt lgkmcnt(6)\n"
  "v_mfma_f32_16x16x32_bf16 v[140:143], v[40:43], v[52:55], v[140:143]\n"
  "ds_read_b64_tr_b16 v[36:37], v3 offset:57344\n"
  "v_mfma_f32_16x16x32_bf16 v[136:139], v[40:43], v[48:51], v[136:139]\n"
  "ds_read_b64_tr_b16 v[38:39], v3 offset:57856\n"
  "s_waitcnt lgkmcnt(6)\n"
  "v_mfma_f32_16x16x32_bf16 v[144:147], v[44:47], v[48:51], v[144:147]\n"
  "ds_read_b64_tr_b16 v[40:41], v4 offset:57344\n"
  "v_mfma_f32_16x16x32_bf16 v[148:151], v[44:47], v[52:55], v[148:151]\n"
  "ds_read_b64_tr_b16 v[42:43], v4 offset:57856\n"
  "s_waitcnt lgkmcnt(6)\n"
  "v_mfma_f32_16x16x32_bf16 v[156:159], v[28:31], v[52:55], v[156:159]\n"
  "ds_read_b64_tr_b16 v[44:45], v5 offset:57344\n"
  "v_mfma_f32_16x16x32_bf16 v[152:155], v[28:31], v[48:51], v[152:155]\n"
  "ds_read_b64_tr_b16 v[46:47], v5 offset:57856\n"
  "s_waitcnt lgkmcnt(6)\n"
  "v_mfma_f32_16x16x32_bf16 v[160:163], v[32:35], v[48:51], v[160:163]\n"
  "ds_read_b64_tr_b16 v[28:29], v2 offset:53248\n"
  "v_mfma_f32_16x16x32_bf16 v[164:167], v[32:35], v[52:55], v[164:167]\n"
  "ds_read_b64_tr_b16 v[30:31], v2 offset:53760\n"
  "s_waitcnt lgkmcnt(6)\n"
  "v_mfma_f32_16x16x32_bf16 v[172:175], v[36:39], v[52:55], v[172:175]\n"
  "ds_read_b64_tr_b16 v[32:33], v3 offset:53248\n"
  "v_mfma_f32_16x16x32_bf16 v[168:171], v[36:39], v[48:51], v[168:171]\n"
  "ds_read_b64_tr_b16 v[34:35], v3 offset:53760\n"
  "s_waitcnt lgkmcnt(6)\n"
  "v_mfma_f32_16x16x32_bf16 v[176:179], v[40:43], v[48:51], v[176:179]\n"
  "ds_read_b64_tr_b16 v[36:37], v4 offset:53248\n"
  "v_mfma_f32_16x16x32_bf16 v[180:183], v[40:43], v[52:55], v[180:183]\n"
  "ds_read_b64_tr_b16 v[38:39], v4 offset:53760\n"
  "s_waitcnt lgkmcnt(6)\n"
  "v_mfma_f32_16x16x32_bf16 v[188:191], v[44:47], v[52:55], v[188:191]\n"
  "ds_read_b64_tr_b16 v[40:41], v5 offset:53248\n"
  "v_mfma_f32_16x16x32_bf16 v[184:187], v[44:47], v[48:51], v[184:187]\n"
  "ds_read_b64_tr_b16 v[42:43], v5 offset:53760\n"
  "s_waitcnt lgkmcnt(6)\n"
  "v_mfma_f32_16x16x32_bf16 v[128:131], v[28:31], v[56:59], v[128:131]\n"
  "ds_read_b64_tr_b16 v[44:45], v2 offset:61440\n"
  "v_mfma_f32_16x16x32_bf16 v[132:135], v[28:31], v[60:63], v[132:135]\n"
  "ds_read_b64_tr_b16 v[46:47], v2 offset:61952\n"
  "s_waitcnt lgkmcnt(6)\n"
  "v_mfma_f32_16x16x32_bf16 v[140:143], v[32:35], v[60:63], v[140:143]\n"
  "ds_read_b64_tr_b16 v[28:29], v3 offset:61440\n"
  "v_mfma_f32_16x16x32_bf16 v[136:139], v[32:35], v[56:59], v[136:139]\n"
  "ds_read_b64_tr_b16 v[30:31], v3 offset:61952\n"
  "s_waitcnt lgkmcnt(6)\n"
  "v_mfma_f32_16x16x32_bf16 v[144:147], v[36:39], v[56:59], v[144:147]\n"
  "ds_read_b64_tr_b16 v[32:33], v4 offset:61440\n"
  "v_mfma_f32_16x16x32_bf16 v[148:151], v[36:39], v[60:63], v[148:151]\n"
  "ds_read_b64_tr_b16 v[34:35], v4 offset:61952\n"
  "s_waitcnt lgkmcnt(6)\n"
  "v_mfma_f32_16x16x32_bf16 v[156:159], v[40:43], v[60:63], v[156:159]\n"
  "ds_read_b64_tr_b16 v[36:37], v5 offset:61440\n"
  "v_mfma_f32_16x16x32_bf16 v[152:155], v[40:43], v[56:59], v[152:155]\n"
  "ds_read_b64_tr_b16 v[38:39], v5 offset:61952\n"
  "s_waitcnt lgkmcnt(6)\n"
  "v_mfma_f32_16x16x32_bf16 v[160:163], v[44:47], v[56:59], v[160:163]\n"
  "s_barrier\n"
  "ds_read_b64_tr_b16 v[40:41], v2 offset:0\n"
  "v_mfma_f32_16x16x32_bf16 v[164:167], v[44:47], v[60:63], v[164:167]\n"
  "ds_read_b64_tr_b16 v[42:43], v2 offset:512\n"
  "ds_read_b128 v[48:51], v10 offset:0\n"
  "ds_read_b128 v[52:55], v10 offset:2048\n"
  "s_waitcnt lgkmcnt(8)\n"
  "v_mfma_f32_16x16x32_bf16 v[172:175], v[28:31], v[60:63], v[172:175]\n"
  "ds_read_b64_tr_b16 v[44:45], v3 offset:0\n"
  "v_mfma_f32_16x16x32_bf16 v[168:171], v[28:31], v[56:59], v[168:171]\n"
  "ds_read_b64_tr_b16 v[46:47], v3 offset:512\n"
  "s_waitcnt lgkmcnt(8)\n"
  "v_mfma_f32_16x16x32_bf16 v[176:179], v[32:35], v[56:59], v[176:179]\n"
  "ds_read_b64_tr_b16 v[28:29], v4 offset:0\n"
  "v_mfma_f32_16x16x32_bf16 v[180:183], v[32:35], v[60:63], v[180:183]\n"
  "ds_read_b64_tr_b16 v[30:31], v4 offset:512\n"
  "s_waitcnt lgkmcnt(8)\n"
  "v_mfma_f32_16x16x32_bf16 v[188:191], v[36:39], v[60:63], v[188:191]\n"
  "ds_read_b64_tr_b16 v[32:33], v5 offset:0\n"
  "v_mfma_f32_16x16x32_bf16 v[184:187], v[36:39], v[56:59], v[184:187]\n"
  "ds_read_b64_tr_b16 v[34:35], v5 offset:512\n"
  "s_waitcnt lgkmcnt(7)\n"
  "v_mfma_f32_16x16x32_bf16 v[64:67], v[40:43], v[48:51], v[64:67]\n"
  "ds_read_b64_tr_b16 v[36:37], v2 offset:8192\n"
  "s_waitcnt lgkmcnt(7)\n"
  "v_mfma_f32_16x16x32_bf16 v[68:71], v[40:43], v[52:55], v[68:71]\n"
  "ds_read_b64_tr_b16 v[38:39], v2 offset:8704\n"
  "ds_read_b128 v[56:59], v11 offset:0\n"
  "ds_read_b128 v[60:63], v11 offset:2048\n"
  "s_waitcnt lgkmcnt(8)\n"
  "v_mfma_f32_16x16x32_bf16 v[76:79], v[44:47], v[52:55], v[76:79]\n"
  "ds_read_b64_tr_b16 v[40:41], v3 offset:8192\n"
  "v_mfma_f32_16x16x32_bf16 v[72:75], v[44:47], v[48:51], v[72:75]\n"
  "ds_read_b64_tr_b16 v[42:43], v3 offset:8704\n"
  "s_waitcnt lgkmcnt(8)\n"
  "v_mfma_f32_16x16x32_bf16 v[80:83], v[28:31], v[48:51], v[80:83]\n"
  "ds_read_b64_tr_b16 v[44:45], v4 offset:8192\n"
  "v_mfma_f32_16x16x32_bf16 v[84:87], v[28:31], v[52:55], v[84:87]\n"
  "ds_read_b64_tr_b16 v[46:47], v4 offset:8704\n"
  "s_waitcnt lgkmcnt(8)\n"
  "v_mfma_f32_16x16x32_bf16 v[92:95], v[32:35], v[52:55], v[92:95]\n"
  "ds_read_b64_tr_b16 v[28:29], v5 offset:8192\n"
  "v_mfma_f32_16x16x32_bf16 v[88:91], v[32:35], v[48:51], v[88:91]\n"
  "ds_read_b64_tr_b16 v[30:31], v5 offset:8704\n"
  "s_waitcnt lgkmcnt(8)\n"
  "v_mfma_f32_16x16x32_bf16 v[96:99], v[36:39], v[48:51], v[96:99]\n"
  "ds_read_b64_tr_b16 v[32:33], v2 offset:4096\n"
  "v_mfma_f32_16x16x32_bf16 v[100:103], v[36:39], v[52:55], v[100:103]\n"
  "ds_read_b64_tr_b16 v[34:35], v2 offset:4608\n"
  "s_waitcnt lgkmcnt(6)\n"
  "v_mfma_f32_16x16x32_bf16 v[108:111], v[40:43], v[52:55], v[108:111]\n"
  "ds_read_b64_tr_b16 v[36:37], v3 offset:4096\n"
  "v_mfma_f32_16x16x32_bf16 v[104:107], v[40:43], v[48:51], v[104:107]\n"
  "ds_read_b64_tr_b16 v[38:39], v3 offset:4608\n"
  "s_waitcnt lgkmcnt(6)\n"
  "v_mfma_f32_16x16x32_bf16 v[112:115], v[44:47], v[48:51], v[112:115]\n"
  "ds_read_b64_tr_b16 v[40:41], v4 offset:4096\n"
  "v_mfma_f32_16x16x32_bf16 v[116:119], v[44:47], v[52:55], v[116:119]\n"
  "ds_read_b64_tr_b16 v[42:43], v4 offset:4608\n"
  "s_waitcnt lgkmcnt(6)\n"
  "v_mfma_f32_16x16x32_bf16 v[124:127], v[28:31], v[52:55], v[124:127]\n"
  "ds_read_b64_tr_b16 v[44:45], v5 offset:4096\n"
  "v_mfma_f32_16x16x32_bf16 v[120:123], v[28:31], v[48:51], v[120:123]\n"
  "ds_read_b64_tr_b16 v[46:47], v5 offset:4608\n"
  "s_waitcnt lgkmcnt(6)\n"
  "v_mfma_f32_16x16x32_bf16 v[64:67], v[32:35], v[56:59], v[64:67]\n"
  "ds_read_b64_tr_b16 v[28:29], v2 offset:12288\n"
  "v_mfma_f32_16x16x32_bf16 v[68:71], v[32:35], v[60:63], v[68:71]\n"
  "ds_read_b64_tr_b16 v[30:31], v2 offset:12800\n"
  "s_waitcnt lgkmcnt(6)\n"
  "v_mfma_f32_16x16x32_bf16 v[76:79], v[36:39], v[60:63], v[76:79]\n"
  "ds_read_b64_tr_b16 v[32:33], v3 offset:12288\n"
  "v_mfma_f32_16x16x32_bf16 v[72:75], v[36:39], v[56:59], v[72:75]\n"
  "ds_read_b64_tr_b16 v[34:35], v3 offset:12800\n"
  "s_waitcnt lgkmcnt(6)\n"
  "v_mfma_f32_16x16x32_bf16 v[80:83], v[40:43], v[56:59], v[80:83]\n"
  "ds_read_b64_tr_b16 v[36:37], v4 offset:12288\n"
  "v_mfma_f32_16x16x32_bf16 v[84:87], v[40:43], v[60:63], v[84:87]\n"
  "ds_read_b64_tr_b16 v[38:39], v4 offset:12800\n"
  "s_waitcnt lgkmcnt(6)\n"
  "v_mfma_f32_16x16x32_bf16 v[92:95], v[44:47], v[60:63], v[92:95]\n"
  "ds_read_b64_tr_b16 v[40:41], v5 offset:12288\n"
  "v_mfma_f32_16x16x32_bf16 v[88:91], v[44:47], v[56:59], v[88:91]\n"
  "ds_read_b64_tr_b16 v[42:43], v5 offset:12800\n"
  "s_waitcnt lgkmcnt(6)\n"
  "v_mfma_f32_16x16x32_bf16 v[96:99], v[28:31], v[56:59], v[96:99]\n"
  "s_barrier\n"
  "ds_read_b64_tr_b16 v[44:45], v2 offset:16384\n"
  "v_mfma_f32_16x16x32_bf16 v[100:103], v[28:31], v[60:63], v[100:103]\n"
  "ds_read_b64_tr_b16 v[46:47], v2 offset:16896\n"
  "s_waitcnt lgkmcnt(6)\n"
  "v_mfma_f32_16x16x32_bf16 v[108:111], v[32:35], v[60:63], v[108:111]\n"
  "ds_read_b64_tr_b16 v[28:29], v3 offset:16384\n"
  "v_mfma_f32_16x16x32_bf16 v[104:107], v[32:35], v[56:59], v[104:107]\n"
  "ds_read_b64_tr_b16 v[30:31], v3 offset:16896\n"
  "s_waitcnt lgkmcnt(6)\n"
  "v_mfma_f32_16x16x32_bf16 v[112:115], v[36:39], v[56:59], v[112:115]\n"
  "ds_read_b64_tr_b16 v[32:33], v4 offset:16384\n"
  "v_mfma_f32_16x16x32_bf16 v[116:119], v[36:39], v[60:63], v[116:119]\n"
  "ds_read_b64_tr_b16 v[34:35], v4 offset:16896\n"
  "s_waitcnt lgkmcnt(6)\n"
  "v_mfma_f32_16x16x32_bf16 v[124:127], v[40:43], v[60:63], v[124:127]\n"
  "ds_read_b64_tr_b16 v[36:37], v5 offset:16384\n"
  "v_mfma_f32_16x16x32_bf16 v[120:123], v[40:43], v[56:59], v[120:123]\n"
  "ds_read_b64_tr_b16 v[38:39], v5 offset:16896\n"
  "s_waitcnt lgkmcnt(6)\n"
  "v_mfma_f32_16x16x32_bf16 v[128:131], v[44:47], v[48:51], v[128:131]\n"
  "ds_read_b64_tr_b16 v[40:41], v2 offset:24576\n"
  "v_mfma_f32_16x16x32_bf16 v[132:135], v[44:47], v[52:55], v[132:135]\n"
  "ds_read_b64_tr_b16 v[42:43], v2 offset:25088\n"
  "s_waitcnt lgkmcnt(6)\n"
  "v_mfma_f32_16x16x32_bf16 v[140:143], v[28:31], v[52:55], v[140:143]\n"
  "ds_read_b64_tr_b16 v[44:45], v3 offset:24576\n"
  "v_mfma_f32_16x16x32_bf16 v[136:139], v[28:31], v[48:51], v[136:139]\n"
  "ds_read_b64_tr_b16 v[46:47], v3 offset:25088\n"
  "s_waitcnt lgkmcnt(6)\n"
  "v_mfma_f32_16x16x32_bf16 v[144:147], v[32:35], v[48:51], v[144:147]\n"
  "ds_read_b64_tr_b16 v[28:29], v4 offset:24576\n"
  "v_mfma_f32_16x16x32_bf16 v[148:151], v[32:35], v[52:55], v[148:151]\n"
  "ds_read_b64_tr_b16 v[30:31], v4 offset:25088\n"
  "s_waitcnt lgkmcnt(6)\n"
  "v_mfma_f32_16x16x32_bf16 v[156:159], v[36:39], v[52:55], v[156:159]\n"
  "ds_read_b64_tr_b16 v[32:33], v5 offset:24576\n"
  "v_mfma_f32_16x16x32_bf16 v[152:155], v[36:39], v[48:51], v[152:155]\n"
  "ds_read_b64_tr_b16 v[34:35], v5 offset:25088\n"
  "s_waitcnt lgkmcnt(6)\n"
  "v_mfma_f32_16x16x32_bf16 v[160:163], v[40:43], v[48:51], v[160:163]\n"
  "ds_read_b64_tr_b16 v[36:37], v2 offset:20480\n"
  "v_mfma_f32_16x16x32_bf16 v[164:167], v[40:43], v[52:55], v[164:167]\n"
  "ds_read_b64_tr_b16 v[38:39], v2 offset:20992\n"
  "s_waitcnt lgkmcnt(6)\n"
  "v_mfma_f32_16x16x32_bf16 v[172:175], v[44:47], v[52:55], v[172:175]\n"
  "ds_read_b64_tr_b16 v[40:41], v3 offset:20480\n"
  "v_mfma_f32_16x16x32_bf16 v[168:171], v[44:47], v[48:51], v[168:171]\n"
  "ds_read_b64_tr_b16 v[42:43], v3 offset:20992\n"
  "s_waitcnt lgkmcnt(6)\n"
  "v_mfma_f32_16x16x32_bf16 v[176:179], v[28:31], v[48:51], v[176:179]\n"
  "ds_read_b64_tr_b16 v[44:45], v4 offset:20480\n"
  "v_mfma_f32_16x16x32_bf16 v[180:183], v[28:31], v[52:55], v[180:183]\n"
  "ds_read_b64_tr_b16 v[46:47], v4 offset:20992\n"
  "s_waitcnt lgkmcnt(6)\n"
  "v_mfma_f32_16x16x32_bf16 v[188:191], v[32:35], v[52:55], v[188:191]\n"
  "ds_read_b64_tr_b16 v[28:29], v5 offset:20480\n"
  "v_mfma_f32_16x16x32_bf16 v[184:187], v[32:35], v[48:51], v[184:187]\n"
  "ds_read_b64_tr_b16 v[30:31], v5 offset:20992\n"
  "s_waitcnt lgkmcnt(6)\n"
  "v_mfma_f32_16x16x32_bf16 v[128:131], v[36:39], v[56:59], v[128:131]\n"
  "ds_read_b64_tr_b16 v[32:33], v2 offset:28672\n"
  "v_mfma_f32_16x16x32_bf16 v[132:135], v[36:39], v[60:63], v[132:135]\n"
  "ds_read_b64_tr_b16 v[34:35], v2 offset:29184\n"
  "s_waitcnt lgkmcnt(6)\n"
  "v_mfma_f32_16x16x32_bf16 v[140:143], v[40:43], v[60:63], v[140:143]\n"
  "ds_read_b64_tr_b16 v[36:37], v3 offset:28672\n"
  "v_mfma_f32_16x16x32_bf16 v[136:139], v[40:43], v[56:59], v[136:139]\n"
  "ds_read_b64_tr_b16 v[38:39], v3 offset:29184\n"
  "s_waitcnt lgkmcnt(6)\n"
  "v_mfma_f32_16x16x32_bf16 v[144:147], v[44:47], v[56:59], v[144:147]\n"
  "ds_read_b64_tr_b16 v[40:41], v4 offset:28672\n"
  "v_mfma_f32_16x16x32_bf16 v[148:151], v[44:47], v[60:63], v[148:151]\n"
  "ds_read_b64_tr_b16 v[42:43], v4 offset:29184\n"
  "s_waitcnt lgkmcnt(6)\n"
  "v_mfma_f32_16x16x32_bf16 v[156:159], v[28:31], v[60:63], v[156:159]\n"
  "ds_read_b64_tr_b16 v[44:45], v5 offset:28672\n"
  "v_mfma_f32_16x16x32_bf16 v[152:155], v[28:31], v[56:59], v[152:155]\n"
  "ds_read_b64_tr_b16 v[46:47], v5 offset:29184\n"
  "s_waitcnt lgkmcnt(6)\n"
  "v_mfma_f32_16x16x32_bf16 v[160:163], v[32:35], v[56:59], v[160:163]\n"
  "s_barrier\n"
  "ds_read_b64_tr_b16 v[28:29], v2 offset:32768\n"
  "v_mfma_f32_16x16x32_bf16 v[164:167], v[32:35], v[60:63], v[164:167]\n"
  "ds_read_b64_tr_b16 v[30:31], v2 offset:33280\n"
  "ds_read_b128 v[48:51], v10 offset:16384\n"
  "ds_read_b128 v[52:55], v10 offset:18432\n"
  "s_waitcnt lgkmcnt(8)\n"
  "v_mfma_f32_16x16x32_bf16 v[172:175], v[36:39], v[60:63], v[172:175]\n"
  "ds_read_b64_tr_b16 v[32:33], v3 offset:32768\n"
  "v_mfma_f32_16x16x32_bf16 v[168:171], v[36:39], v[56:59], v[168:171]\n"
  "ds_read_b64_tr_b16 v[34:35], v3 offset:33280\n"
  "s_waitcnt lgkmcnt(8)\n"
  "v_mfma_f32_16x16x32_bf16 v[176:179], v[40:43], v[56:59], v[176:179]\n"
  "ds_read_b64_tr_b16 v[36:37], v4 offset:32768\n"
  "v_mfma_f32_16x16x32_bf16 v[180:183], v[40:43], v[60:63], v[180:183]\n"
  "ds_read_b64_tr_b16 v[38:39], v4 offset:33280\n"
  "s_waitcnt lgkmcnt(8)\n"
  "v_mfma_f32_16x16x32_bf16 v[188:191], v[44:47], v[60:63], v[188:191]\n"
  "ds_read_b64_tr_b16 v[40:41], v5 offset:32768\n"
  "v_mfma_f32_16x16x32_bf16 v[184:187], v[44:47], v[56:59], v[184:187]\n"
  "ds_read_b64_tr_b16 v[42:43], v5 offset:33280\n"
  "s_waitcnt lgkmcnt(7)\n"
  "v_mfma_f32_16x16x32_bf16 v[64:67], v[28:31], v[48:51], v[64:67]\n"
  "ds_read_b64_tr_b16 v[44:45], v2 offset:40960\n"
  "s_waitcnt lgkmcnt(7)\n"
  "v_mfma_f32_16x16x32_bf16 v[68:71], v[28:31], v[52:55], v[68:71]\n"
  "ds_read_b64_tr_b16 v[46:47], v2 offset:41472\n"
  "ds_read_b128 v[56:59], v11 offset:16384\n"
  "ds_read_b128 v[60:63], v11 offset:18432\n"
  "s_waitcnt lgkmcnt(8)\n"
  "v_mfma_f32_16x16x32_bf16 v[76:79], v[32:35], v[52:55], v[76:79]\n"
  "ds_read_b64_tr_b16 v[28:29], v3 offset:40960\n"
  "v_mfma_f32_16x16x32_bf16 v[72:75], v[32:35], v[48:51], v[72:75]\n"
  "ds_read_b64_tr_b16 v[30:31], v3 offset:41472\n"
  "s_waitcnt lgkmcnt(8)\n"
  "v_mfma_f32_16x16x32_bf16 v[80:83], v[36:39], v[48:51], v[80:83]\n"
  "ds_read_b64_tr_b16 v[32:33], v4 offset:40960\n"
  "v_mfma_f32_16x16x32_bf16 v[84:87], v[36:39], v[52:55], v[84:87]\n"
  "ds_read_b64_tr_b16 v[34:35], v4 offset:41472\n"
  "s_waitcnt lgkmcnt(8)\n"
  "v_mfma_f32_16x16x32_bf16 v[92:95], v[40:43], v[52:55], v[92:95]\n"
  "ds_read_b64_tr_b16 v[36:37], v5 offset:40960\n"
  "v_mfma_f32_16x16x32_bf16 v[88:91], v[40:43], v[48:51], v[88:91]\n"
  "ds_read_b64_tr_b16 v[38:39], v5 offset:41472\n"
  "s_waitcnt lgkmcnt(8)\n"
  "v_mfma_f32_16x16x32_bf16 v[96:99], v[44:47], v[48:51], v[96:99]\n"
  "ds_read_b64_tr_b16 v[40:41], v2 offset:36864\n"
  "v_mfma_f32_16x16x32_bf16 v[100:103], v[44:47], v[52:55], v[100:103]\n"
  "ds_read_b64_tr_b16 v[42:43], v2 offset:37376\n"
  "s_waitcnt lgkmcnt(6)\n"
  "v_mfma_f32_16x16x32_bf16 v[108:111], v[28:31], v[52:55], v[108:111]\n"
  "ds_read_b64_tr_b16 v[44:45], v3 offset:36864\n"
  "v_mfma_f32_16x16x32_bf16 v[104:107], v[28:31], v[48:51], v[104:107]\n"
  "ds_read_b64_tr_b16 v[46:47], v3 offset:37376\n"
  "s_waitcnt lgkmcnt(6)\n"
  "v_mfma_f32_16x16x32_bf16 v[112:115], v[32:35], v[48:51], v[112:115]\n"
  "ds_read_b64_tr_b16 v[28:29], v4 offset:36864\n"
  "v_mfma_f32_16x16x32_bf16 v[116:119], v[32:35], v[52:55], v[116:119]\n"
  "ds_read_b64_tr_b16 v[30:31], v4 offset:37376\n"
  "s_waitcnt lgkmcnt(6)\n"
  "v_mfma_f32_16x16x32_bf16 v[124:127], v[36:39], v[52:55], v[124:127]\n"
  "ds_read_b64_tr_b16 v[32:33], v5 offset:36864\n"
  "v_mfma_f32_16x16x32_bf16 v[120:123], v[36:39], v[48:51], v[120:123]\n"
  "ds_read_b64_tr_b16 v[34:35], v5 offset:37376\n"
  "s_waitcnt lgkmcnt(6)\n"
  "v_mfma_f32_16x16x32_bf16 v[64:67], v[40:43], v[56:59], v[64:67]\n"
  "ds_read_b64_tr_b16 v[36:37], v2 offset:45056\n"
  "v_mfma_f32_16x16x32_bf16 v[68:71], v[40:43], v[60:63], v[68:71]\n"
  "ds_read_b64_tr_b16 v[38:39], v2 offset:45568\n"
  "s_waitcnt lgkmcnt(6)\n"
  "v_mfma_f32_16x16x32_bf16 v[76:79], v[44:47], v[60:63], v[76:79]\n"
  "ds_read_b64_tr_b16 v[40:41], v3 offset:45056\n"
  "v_mfma_f32_16x16x32_bf16 v[72:75], v[44:47], v[56:59], v[72:75]\n"
  "ds_read_b64_tr_b16 v[42:43], v3 offset:45568\n"
  "s_waitcnt lgkmcnt(6)\n"
  "v_mfma_f32_16x16x32_bf16 v[80:83], v[28:31], v[56:59], v[80:83]\n"
  "ds_read_b64_tr_b16 v[44:45], v4 offset:45056\n"
  "v_mfma_f32_16x16x32_bf16 v[84:87], v[28:31], v[60:63], v[84:87]\n"
  "ds_read_b64_tr_b16 v[46:47], v4 offset:45568\n"
  "s_waitcnt lgkmcnt(6)\n"
  "v_mfma_f32_16x16x32_bf16 v[92:95], v[32:35], v[60:63], v[92:95]\n"
  "ds_read_b64_tr_b16 v[28:29], v5 offset:45056\n"
  "v_mfma_f32_16x16x32_bf16 v[88:91], v[32:35], v[56:59], v[88:91]\n"
  "ds_read_b64_tr_b16 v[30:31], v5 offset:45568\n"
  "s_waitcnt lgkmcnt(6)\n"
  "v_mfma_f32_16x16x32_bf16 v[96:99], v[36:39], v[56:59], v[96:99]\n"
  "s_barrier\n"
  "ds_read_b64_tr_b16 v[32:33], v2 offset:49152\n"
  "v_mfma_f32_16x16x32_bf16 v[100:103], v[36:39], v[60:63], v[100:103]\n"
  "ds_read_b64_tr_b16 v[34:35], v2 offset:49664\n"
  "s_waitcnt lgkmcnt(6)\n"
  "v_mfma_f32_16x16x32_bf16 v[108:111], v[40:43], v[60:63], v[108:111]\n"
  "ds_read_b64_tr_b16 v[36:37], v3 offset:49152\n"
  "v_mfma_f32_16x16x32_bf16 v[104:107], v[40:43], v[56:59], v[104:107]\n"
  "ds_read_b64_tr_b16 v[38:39], v3 offset:49664\n"
  "s_waitcnt lgkmcnt(6)\n"
  "v_mfma_f32_16x16x32_bf16 v[112:115], v[44:47], v[56:59], v[112:115]\n"
  "ds_read_b64_tr_b16 v[40:41], v4 offset:49152\n"
  "v_mfma_f32_16x16x32_bf16 v[116:119], v[44:47], v[60:63], v[116:119]\n"
  "ds_read_b64_tr_b16 v[42:43], v4 offset:49664\n"
  "s_waitcnt lgkmcnt(6)\n"
  "v_mfma_f32_16x16x32_bf16 v[124:127], v[28:31], v[60:63], v[124:127]\n"
  "ds_read_b64_tr_b16 v[44:45], v5 offset:49152\n"
  "v_mfma_f32_16x16x32_bf16 v[120:123], v[28:31], v[56:59], v[120:123]\n"
  "ds_read_b64_tr_b16 v[46:47], v5 offset:49664\n"
  "s_waitcnt lgkmcnt(6)\n"
  "v_mfma_f32_16x16x32_bf16 v[128:131], v[32:35], v[48:51], v[128:131]\n"
  "ds_read_b64_tr_b16 v[28:29], v2 offset:57344\n"
  "v_mfma_f32_16x16x32_bf16 v[132:135], v[32:35], v[52:55], v[132:135]\n"
  "ds_read_b64_tr_b16 v[30:31], v2 offset:57856\n"
  "s_waitcnt lgkmcnt(6)\n"
  "v_mfma_f32_16x16x32_bf16 v[140:143], v[36:39], v[52:55], v[140:143]\n"
  "ds_read_b64_tr_b16 v[32:33], v3 offset:57344\n"
  "v_mfma_f32_16x16x32_bf16 v[136:139], v[36:39], v[48:51], v[136:139]\n"
  "ds_read_b64_tr_b16 v[34:35], v3 offset:57856\n"
  "s_waitcnt lgkmcnt(6)\n"
  "v_mfma_f32_16x16x32_bf16 v[144:147], v[40:43], v[48:51], v[144:147]\n"
  "ds_read_b64_tr_b16 v[36:37], v4 offset:57344\n"
  "v_mfma_f32_16x16x32_bf16 v[148:151], v[40:43], v[52:55], v[148:151]\n"
  "ds_read_b64_tr_b16 v[38:39], v4 offset:57856\n"
  "s_waitcnt lgkmcnt(6)\n"
  "v_mfma_f32_16x16x32_bf16 v[156:159], v[44:47], v[52:55], v[156:159]\n"
  "ds_read_b64_tr_b16 v[40:41], v5 offset:57344\n"
  "v_mfma_f32_16x16x32_bf16 v[152:155], v[44:47], v[48:51], v[152:155]\n"
  "ds_read_b64_tr_b16 v[42:43], v5 offset:57856\n"
  "s_waitcnt lgkmcnt(6)\n"
  "v_mfma_f32_16x16x32_bf16 v[160:163], v[28:31], v[48:51], v[160:163]\n"
  "ds_read_b64_tr_b16 v[44:45], v2 offset:53248\n"
  "v_mfma_f32_16x16x32_bf16 v[164:167], v[28:31], v[52:55], v[164:167]\n"
  "ds_read_b64_tr_b16 v[46:47], v2 offset:53760\n"
  "s_waitcnt lgkmcnt(6)\n"
  "v_mfma_f32_16x16x32_bf16 v[172:175], v[32:35], v[52:55], v[172:175]\n"
  "ds_read_b64_tr_b16 v[28:29], v3 offset:53248\n"
  "v_mfma_f32_16x16x32_bf16 v[168:171], v[32:35], v[48:51], v[168:171]\n"
  "ds_read_b64_tr_b16 v[30:31], v3 offset:53760\n"
  "s_waitcnt lgkmcnt(6)\n"
  "v_mfma_f32_16x16x32_bf16 v[176:179], v[36:39], v[48:51], v[176:179]\n"
  "ds_read_b64_tr_b16 v[32:33], v4 offset:53248\n"
  "v_mfma_f32_16x16x32_bf16 v[180:183], v[36:39], v[52:55], v[180:183]\n"
  "ds_read_b64_tr_b16 v[34:35], v4 offset:53760\n"
  "s_waitcnt lgkmcnt(6)\n"
  "v_mfma_f32_16x16x32_bf16 v[188:191], v[40:43], v[52:55], v[188:191]\n"
  "ds_read_b64_tr_b16 v[36:37], v5 offset:53248\n"
  "v_mfma_f32_16x16x32_bf16 v[184:187], v[40:43], v[48:51], v[184:187]\n"
  "ds_read_b64_tr_b16 v[38:39], v5 offset:53760\n"
  "s_waitcnt lgkmcnt(6)\n"
  "v_mfma_f32_16x16x32_bf16 v[128:131], v[44:47], v[56:59], v[128:131]\n"
  "ds_read_b64_tr_b16 v[40:41], v2 offset:61440\n"
  "v_mfma_f32_16x16x32_bf16 v[132:135], v[44:47], v[60:63], v[132:135]\n"
  "ds_read_b64_tr_b16 v[42:43], v2 offset:61952\n"
  "s_waitcnt lgkmcnt(6)\n"
  "v_mfma_f32_16x16x32_bf16 v[140:143], v[28:31], v[60:63], v[140:143]\n"
  "ds_read_b64_tr_b16 v[44:45], v3 offset:61440\n"
  "v_mfma_f32_16x16x32_bf16 v[136:139], v[28:31], v[56:59], v[136:139]\n"
  "ds_read_b64_tr_b16 v[46:47], v3 offset:61952\n"
  "s_waitcnt lgkmcnt(6)\n"
  "v_mfma_f32_16x16x32_bf16 v[144:147], v[32:35], v[56:59], v[144:147]\n"
  "ds_read_b64_tr_b16 v[28:29], v4 offset:61440\n"
  "v_mfma_f32_16x16x32_bf16 v[148:151], v[32:35], v[60:63], v[148:151]\n"
  "ds_read_b64_tr_b16 v[30:31], v4 offset:61952\n"
  "s_waitcnt lgkmcnt(6)\n"
  "v_mfma_f32_16x16x32_bf16 v[156:159], v[36:39], v[60:63], v[156:159]\n"
  "ds_read_b64_tr_b16 v[32:33], v5 offset:61440\n"
  "v_mfma_f32_16x16x32_bf16 v[152:155], v[36:39], v[56:59], v[152:155]\n"
  "ds_read_b64_tr_b16 v[34:35], v5 offset:61952\n"
  "s_waitcnt lgkmcnt(6)\n"
  "v_mfma_f32_16x16x32_bf16 v[160:163], v[40:43], v[56:59], v[160:163]\n"
  "s_barrier\n"
  "ds_read_b64_tr_b16 v[36:37], v2 offset:0\n"
  "v_mfma_f32_16x16x32_bf16 v[164:167], v[40:43], v[60:63], v[164:167]\n"
  "ds_read_b64_tr_b16 v[38:39], v2 offset:512\n"
  "ds_read_b128 v[48:51], v10 offset:0\n"
  "ds_read_b128 v[52:55], v10 offset:2048\n"
  "s_waitcnt lgkmcnt(8)\n"
  "v_mfma_f32_16x16x32_bf16 v[172:175], v[44:47], v[60:63], v[172:175]\n"
  "ds_read_b64_tr_b16 v[40:41], v3 offset:0\n"
  "v_mfma_f32_16x16x32_bf16 v[168:171], v[44:47], v[56:59], v[168:171]\n"
  "ds_read_b64_tr_b16 v[42:43], v3 offset:512\n"
  "s_waitcnt lgkmcnt(8)\n"
  "v_mfma_f32_16x16x32_bf16 v[176:179], v[28:31], v[56:59], v[176:179]\n"
  "ds_read_b64_tr_b16 v[44:45], v4 offset:0\n"
  "v_mfma_f32_16x16x32_bf16 v[180:183], v[28:31], v[60:63], v[180:183]\n"
  "ds_read_b64_tr_b16 v[46:47], v4 offset:512\n"
  "s_waitcnt lgkmcnt(8)\n"
  "v_mfma_f32_16x16x32_bf16 v[188:191], v[32:35], v[60:63], v[188:191]\n"
  "ds_read_b64_tr_b16 v[28:29], v5 offset:0\n"
  "v_mfma_f32_16x16x32_bf16 v[184:187], v[32:35], v[56:59], v[184:187]\n"
  "ds_read_b64_tr_b16 v[30:31], v5 offset:512\n"
  "s_waitcnt lgkmcnt(7)\n"
  "v_mfma_f32_16x16x32_bf16 v[64:67], v[36:39], v[48:51], v[64:67]\n"
  "ds_read_b64_tr_b16 v[32:33], v2 offset:8192\n"
  "s_waitcnt lgkmcnt(7)\n"
  "v_mfma_f32_16x16x32_bf16 v[68:71], v[36:39], v[52:55], v[68:71]\n"
  "ds_read_b64_tr_b16 v[34:35], v2 offset:8704\n"
  "ds_read_b128 v[56:59], v11 offset:0\n"
  "ds_read_b128 v[60:63], v11 offset:2048\n"
  "s_waitcnt lgkmcnt(8)\n"
  "v_mfma_f32_16x16x32_bf16 v[76:79], v[40:43], v[52:55], v[76:79]\n"
  "ds_read_b64_tr_b16 v[36:37], v3 offset:8192\n"
  "v_mfma_f32_16x16x32_bf16 v[72:75], v[40:43], v[48:51], v[72:75]\n"
  "ds_read_b64_tr_b16 v[38:39], v3 offset:8704\n"
  "s_waitcnt lgkmcnt(8)\n"
  "v_mfma_f32_16x16x32_bf16 v[80:83], v[44:47], v[48:51], v[80:83]\n"
  "ds_read_b64_tr_b16 v[40:41], v4 offset:8192\n"
  "v_mfma_f32_16x16x32_bf16 v[84:87], v[44:47], v[52:55], v[84:87]\n"
  "ds_read_b64_tr_b16 v[42:43], v4 offset:8704\n"
  "s_waitcnt lgkmcnt(8)\n"
  "v_mfma_f32_16x16x32_bf16 v[92:95], v[28:31], v[52:55], v[92:95]\n"
  "ds_read_b64_tr_b16 v[44:45], v5 offset:8192\n"
  "v_mfma_f32_16x16x32_bf16 v[88:91], v[28:31], v[48:51], v[88:91]\n"
  "ds_read_b64_tr_b16 v[46:47], v5 offset:8704\n"
  "s_waitcnt lgkmcnt(8)\n"
  "v_mfma_f32_16x16x32_bf16 v[96:99], v[32:35], v[48:51], v[96:99]\n"
  "ds_read_b64_tr_b16 v[28:29], v2 offset:4096\n"
  "v_mfma_f32_16x16x32_bf16 v[100:103], v[32:35], v[52:55], v[100:103]\n"
  "ds_read_b64_tr_b16 v[30:31], v2 offset:4608\n"
  "s_waitcnt lgkmcnt(6)\n"
  "v_mfma_f32_16x16x32_bf16 v[108:111], v[36:39], v[52:55], v[108:111]\n"
  "ds_read_b64_tr_b16 v[32:33], v3 offset:4096\n"
  "v_mfma_f32_16x16x32_bf16 v[104:107], v[36:39], v[48:51], v[104:107]\n"
  "ds_read_b64_tr_b16 v[34:35], v3 offset:4608\n"
  "s_waitcnt lgkmcnt(6)\n"
  "v_mfma_f32_16x16x32_bf16 v[112:115], v[40:43], v[48:51], v[112:115]\n"
  "ds_read_b64_tr_b16 v[36:37], v4 offset:4096\n"
  "v_mfma_f32_16x16x32_bf16 v[116:119], v[40:43], v[52:55], v[116:119]\n"
  "ds_read_b64_tr_b16 v[38:39], v4 offset:4608\n"
  "s_waitcnt lgkmcnt(6)\n"
  "v_mfma_f32_16x16x32_bf16 v[124:127], v[44:47], v[52:55], v[124:127]\n"
  "ds_read_b64_tr_b16 v[40:41], v5 offset:4096\n"
  "v_mfma_f32_16x16x32_bf16 v[120:123], v[44:47], v[48:51], v[120:123]\n"
  "ds_read_b64_tr_b16 v[42:43], v5 offset:4608\n"
  "s_waitcnt lgkmcnt(6)\n"
  "v_mfma_f32_16x16x32_bf16 v[64:67], v[28:31], v[56:59], v[64:67]\n"
  "ds_read_b64_tr_b16 v[44:45], v2 offset:12288\n"
  "v_mfma_f32_16x16x32_bf16 v[68:71], v[28:31], v[60:63], v[68:71]\n"
  "ds_read_b64_tr_b16 v[46:47], v2 offset:12800\n"
  "s_waitcnt lgkmcnt(6)\n"
  "v_mfma_f32_16x16x32_bf16 v[76:79], v[32:35], v[60:63], v[76:79]\n"
  "ds_read_b64_tr_b16 v[28:29], v3 offset:12288\n"
  "v_mfma_f32_16x16x32_bf16 v[72:75], v[32:35], v[56:59], v[72:75]\n"
  "ds_read_b64_tr_b16 v[30:31], v3 offset:12800\n"
  "s_waitcnt lgkmcnt(6)\n"
  "v_mfma_f32_16x16x32_bf16 v[80:83], v[36:39], v[56:59], v[80:83]\n"
  "ds_read_b64_tr_b16 v[32:33], v4 offset:12288\n"
  "v_mfma_f32_16x16x32_bf16 v[84:87], v[36:39], v[60:63], v[84:87]\n"
  "ds_read_b64_tr_b16 v[34:35], v4 offset:12800\n"
  "s_waitcnt lgkmcnt(6)\n"
  "v_mfma_f32_16x16x32_bf16 v[92:95], v[40:43], v[60:63], v[92:95]\n"
  "ds_read_b64_tr_b16 v[36:37], v5 offset:12288\n"
  "v_mfma_f32_16x16x32_bf16 v[88:91], v[40:43], v[56:59], v[88:91]\n"
  "ds_read_b64_tr_b16 v[38:39], v5 offset:12800\n"
  "s_waitcnt lgkmcnt(6)\n"
  "v_mfma_f32_16x16x32_bf16 v[96:99], v[44:47], v[56:59], v[96:99]\n"
  "s_barrier\n"
  "ds_read_b64_tr_b16 v[40:41], v2 offset:16384\n"
  "v_mfma_f32_16x16x32_bf16 v[100:103], v[44:47], v[60:63], v[100:103]\n"
  "ds_read_b64_tr_b16 v[42:43], v2 offset:16896\n"
  "ds_read_b128 v[48:51], v10 offset:16384\n"
  "ds_read_b128 v[52:55], v10 offset:18432\n"
  "s_waitcnt lgkmcnt(8)\n"
  "v_mfma_f32_16x16x32_bf16 v[108:111], v[28:31], v[60:63], v[108:111]\n"
  "ds_read_b64_tr_b16 v[44:45], v3 offset:16384\n"
  "v_mfma_f32_16x16x32_bf16 v[104:107], v[28:31], v[56:59], v[104:107]\n"
  "ds_read_b64_tr_b16 v[46:47], v3 offset:16896\n"
  "s_waitcnt lgkmcnt(8)\n"
  "v_mfma_f32_16x16x32_bf16 v[112:115], v[32:35], v[56:59], v[112:115]\n"
  "ds_read_b64_tr_b16 v[28:29], v4 offset:16384\n"
  "v_mfma_f32_16x16x32_bf16 v[116:119], v[32:35], v[60:63], v[116:119]\n"
  "ds_read_b64_tr_b16 v[30:31], v4 offset:16896\n"
  "s_waitcnt lgkmcnt(8)\n"
  "v_mfma_f32_16x16x32_bf16 v[124:127], v[36:39], v[60:63], v[124:127]\n"
  "ds_read_b64_tr_b16 v[32:33], v5 offset:16384\n"
  "v_mfma_f32_16x16x32_bf16 v[120:123], v[36:39], v[56:59], v[120:123]\n"
  "ds_read_b64_tr_b16 v[34:35], v5 offset:16896\n"
  "s_waitcnt lgkmcnt(7)\n"
  "v_mfma_f32_16x16x32_bf16 v[64:67], v[40:43], v[48:51], v[64:67]\n"
  "ds_read_b64_tr_b16 v[36:37], v2 offset:24576\n"
  "s_waitcnt lgkmcnt(7)\n"
  "v_mfma_f32_16x16x32_bf16 v[68:71], v[40:43], v[52:55], v[68:71]\n"
  "ds_read_b64_tr_b16 v[38:39], v2 offset:25088\n"
  "ds_read_b128 v[56:59], v11 offset:16384\n"
  "ds_read_b128 v[60:63], v11 offset:18432\n"
  "s_waitcnt lgkmcnt(8)\n"
  "v_mfma_f32_16x16x32_bf16 v[76:79], v[44:47], v[52:55], v[76:79]\n"
  "ds_read_b64_tr_b16 v[40:41], v3 offset:24576\n"
  "v_mfma_f32_16x16x32_bf16 v[72:75], v[44:47], v[48:51], v[72:75]\n"
  "ds_read_b64_tr_b16 v[42:43], v3 offset:25088\n"
  "s_waitcnt lgkmcnt(8)\n"
  "v_mfma_f32_16x16x32_bf16 v[80:83], v[28:31], v[48:51], v[80:83]\n"
  "ds_read_b64_tr_b16 v[44:45], v4 offset:24576\n"
  "v_mfma_f32_16x16x32_bf16 v[84:87], v[28:31], v[52:55], v[84:87]\n"
  "ds_read_b64_tr_b16 v[46:47], v4 offset:25088\n"
  "s_waitcnt lgkmcnt(8)\n"
  "v_mfma_f32_16x16x32_bf16 v[92:95], v[32:35], v[52:55], v[92:95]\n"
  "ds_read_b64_tr_b16 v[28:29], v5 offset:24576\n"
  "v_mfma_f32_16x16x32_bf16 v[88:91], v[32:35], v[48:51], v[88:91]\n"
  "ds_read_b64_tr_b16 v[30:31], v5 offset:25088\n"
  "s_waitcnt lgkmcnt(8)\n"
  "v_mfma_f32_16x16x32_bf16 v[96:99], v[36:39], v[48:51], v[96:99]\n"
  "ds_read_b64_tr_b16 v[32:33], v2 offset:20480\n"
  "v_mfma_f32_16x16x32_bf16 v[100:103], v[36:39], v[52:55], v[100:103]\n"
  "ds_read_b64_tr_b16 v[34:35], v2 offset:20992\n"
  "s_waitcnt lgkmcnt(6)\n"
  "v_mfma_f32_16x16x32_bf16 v[108:111], v[40:43], v[52:55], v[108:111]\n"
  "ds_read_b64_tr_b16 v[36:37], v3 offset:20480\n"
  "v_mfma_f32_16x16x32_bf16 v[104:107], v[40:43], v[48:51], v[104:107]\n"
  "ds_read_b64_tr_b16 v[38:39], v3 offset:20992\n"
  "s_waitcnt lgkmcnt(6)\n"
  "v_mfma_f32_16x16x32_bf16 v[112:115], v[44:47], v[48:51], v[112:115]\n"
  "ds_read_b64_tr_b16 v[40:41], v4 offset:20480\n"
  "v_mfma_f32_16x16x32_bf16 v[116:119], v[44:47], v[52:55], v[116:119]\n"
  "ds_read_b64_tr_b16 v[42:43], v4 offset:20992\n"
  "s_waitcnt lgkmcnt(6)\n"
  "v_mfma_f32_16x16x32_bf16 v[124:127], v[28:31], v[52:55], v[124:127]\n"
  "ds_read_b64_tr_b16 v[44:45], v5 offset:20480\n"
  "v_mfma_f32_16x16x32_bf16 v[120:123], v[28:31], v[48:51], v[120:123]\n"
  "ds_read_b64_tr_b16 v[46:47], v5 offset:20992\n"
  "s_waitcnt lgkmcnt(6)\n"
  "v_mfma_f32_16x16x32_bf16 v[64:67], v[32:35], v[56:59], v[64:67]\n"
  "ds_read_b64_tr_b16 v[28:29], v2 offset:28672\n"
  "v_mfma_f32_16x16x32_bf16 v[68:71], v[32:35], v[60:63], v[68:71]\n"
  "ds_read_b64_tr_b16 v[30:31], v2 offset:29184\n"
  "s_waitcnt lgkmcnt(6)\n"
  "v_mfma_f32_16x16x32_bf16 v[76:79], v[36:39], v[60:63], v[76:79]\n"
  "ds_read_b64_tr_b16 v[32:33], v3 offset:28672\n"
  "v_mfma_f32_16x16x32_bf16 v[72:75], v[36:39], v[56:59], v[72:75]\n"
  "ds_read_b64_tr_b16 v[34:35], v3 offset:29184\n"
  "s_waitcnt lgkmcnt(6)\n"
  "v_mfma_f32_16x16x32_bf16 v[80:83], v[40:43], v[56:59], v[80:83]\n"
  "ds_read_b64_tr_b16 v[36:37], v4 offset:28672\n"
  "v_mfma_f32_16x16x32_bf16 v[84:87], v[40:43], v[60:63], v[84:87]\n"
  "ds_read_b64_tr_b16 v[38:39], v4 offset:29184\n"
  "s_waitcnt lgkmcnt(6)\n"
  "v_mfma_f32_16x16x32_bf16 v[92:95], v[44:47], v[60:63], v[92:95]\n"
  "ds_read_b64_tr_b16 v[40:41], v5 offset:28672\n"
  "v_mfma_f32_16x16x32_bf16 v[88:91], v[44:47], v[56:59], v[88:91]\n"
  "ds_read_b64_tr_b16 v[42:43], v5 offset:29184\n"
  "v_cvt_pk_bf16_f32 v192, v64, v65\n"
  "v_cvt_pk_bf16_f32 v193, v66, v67\n"
  "v_cvt_pk_bf16_f32 v194, v72, v73\n"
  "v_cvt_pk_bf16_f32 v195, v74, v75\n"
  "s_waitcnt lgkmcnt(6)\n"
  "v_mfma_f32_16x16x32_bf16 v[96:99], v[28:31], v[56:59], v[96:99]\n"
  "s_barrier\n"
  "v_add_u32 v13, 65536, v13\n"
  "v_xor_b32 v14, 0, v13\n"
  "v_xor_b32 v15, 32, v13\n"
  "ds_read_b64 v[44:45], v14 offset:32768\n"
  "v_mfma_f32_16x16x32_bf16 v[100:103], v[28:31], v[60:63], v[100:103]\n"
  "ds_read_b64 v[46:47], v15 offset:32768\n"
  "v_cvt_pk_bf16_f32 v196, v68, v69\n"
  "v_cvt_pk_bf16_f32 v197, v70, v71\n"
  "v_cvt_pk_bf16_f32 v198, v76, v77\n"
  "v_cvt_pk_bf16_f32 v199, v78, v79\n"
  "s_waitcnt lgkmcnt(6)\n"
  "v_mfma_f32_16x16x32_bf16 v[108:111], v[32:35], v[60:63], v[108:111]\n"
  "ds_read_b64 v[28:29], v14 offset:36864\n"
  "v_mfma_f32_16x16x32_bf16 v[104:107], v[32:35], v[56:59], v[104:107]\n"
  "ds_read_b64 v[30:31], v15 offset:36864\n"
  "v_cvt_pk_bf16_f32 v200, v80, v81\n"
  "v_cvt_pk_bf16_f32 v201, v82, v83\n"
  "v_cvt_pk_bf16_f32 v202, v88, v89\n"
  "v_cvt_pk_bf16_f32 v203, v90, v91\n"
  "s_waitcnt lgkmcnt(6)\n"
  "v_mfma_f32_16x16x32_bf16 v[112:115], v[36:39], v[56:59], v[112:115]\n"
  "ds_read_b64 v[32:33], v14 offset:40960\n"
  "v_mfma_f32_16x16x32_bf16 v[116:119], v[36:39], v[60:63], v[116:119]\n"
  "ds_read_b64 v[34:35], v15 offset:40960\n"
  "v_cvt_pk_bf16_f32 v204, v84, v85\n"
  "v_cvt_pk_bf16_f32 v205, v86, v87\n"
  "v_cvt_pk_bf16_f32 v206, v92, v93\n"
  "v_cvt_pk_bf16_f32 v207, v94, v95\n"
  "s_waitcnt lgkmcnt(6)\n"
  "v_mfma_f32_16x16x32_bf16 v[124:127], v[40:43], v[60:63], v[124:127]\n"
  "ds_read_b64 v[36:37], v14 offset:45056\n"
  "v_mfma_f32_16x16x32_bf16 v[120:123], v[40:43], v[56:59], v[120:123]\n"
  "ds_read_b64 v[38:39], v15 offset:45056\n"
  "v_cvt_pk_bf16_f32 v208, v96, v97\n"
  "v_cvt_pk_bf16_f32 v209, v98, v99\n"
  "v_cvt_pk_bf16_f32 v210, v104, v105\n"
  "v_cvt_pk_bf16_f32 v211, v106, v107\n"
  "v_cvt_pk_bf16_f32 v212, v100, v101\n"
  "v_cvt_pk_bf16_f32 v213, v102, v103\n"
  "v_cvt_pk_bf16_f32 v214, v108, v109\n"
  "v_cvt_pk_bf16_f32 v215, v110, v111\n"
  "v_cvt_pk_bf16_f32 v216, v112, v113\n"
  "v_cvt_pk_bf16_f32 v217, v114, v115\n"
  "v_cvt_pk_bf16_f32 v218, v120, v121\n"
  "v_cvt_pk_bf16_f32 v219, v122, v123\n"
  "v_cvt_pk_bf16_f32 v220, v116, v117\n"
  "v_cvt_pk_bf16_f32 v221, v118, v119\n"
  "v_cvt_pk_bf16_f32 v222, v124, v125\n"
  "v_cvt_pk_bf16_f32 v223, v126, v127\n"
  "s_waitcnt lgkmcnt(6)\n"
  "v_mfma_f32_16x16x32_bf16 v[224:227], v[44:47], v[192:195], 0\n"
  "v_xor_b32 v14, 64, v13\n"
  "v_xor_b32 v15, 96, v13\n"
  "ds_read_b64 v[40:41], v14 offset:32768\n"
  "v_mfma_f32_16x16x32_bf16 v[228:231], v[44:47], v[196:199], 0\n"
  "ds_read_b64 v[42:43], v15 offset:32768\n"
  "s_waitcnt lgkmcnt(6)\n"
  "v_mfma_f32_16x16x32_bf16 v[236:239], v[28:31], v[196:199], 0\n"
  "ds_read_b64 v[44:45], v14 offset:36864\n"
  "v_mfma_f32_16x16x32_bf16 v[232:235], v[28:31], v[192:195], 0\n"
  "ds_read_b64 v[46:47], v15 offset:36864\n"
  "s_waitcnt lgkmcnt(6)\n"
  "v_mfma_f32_16x16x32_bf16 v[240:243], v[32:35], v[192:195], 0\n"
  "ds_read_b64 v[28:29], v14 offset:40960\n"
  "v_mfma_f32_16x16x32_bf16 v[244:247], v[32:35], v[196:199], 0\n"
  "ds_read_b64 v[30:31], v15 offset:40960\n"
  "s_waitcnt lgkmcnt(6)\n"
  "v_mfma_f32_16x16x32_bf16 v[252:255], v[36:39], v[196:199], 0\n"
  "s_barrier\n"
  "ds_read_b64 v[32:33], v14 offset:45056\n"
  "v_mfma_f32_16x16x32_bf16 v[248:251], v[36:39], v[192:195], 0\n"
  "ds_read_b64 v[34:35], v15 offset:45056\n"
  "s_waitcnt lgkmcnt(6)\n"
  "v_mfma_f32_16x16x32_bf16 v[224:227], v[40:43], v[200:203], v[224:227]\n"
  "v_xor_b32 v14, 128, v13\n"
  "v_xor_b32 v15, 160, v13\n"
  "ds_read_b64 v[36:37], v14 offset:32768\n"
  "v_mfma_f32_16x16x32_bf16 v[228:231], v[40:43], v[204:207], v[228:231]\n"
  "ds_read_b64 v[38:39], v15 offset:32768\n"
  "s_waitcnt lgkmcnt(6)\n"
  "v_mfma_f32_16x16x32_bf16 v[236:239], v[44:47], v[204:207], v[236:239]\n"
  "ds_read_b64 v[40:41], v14 offset:36864\n"
  "v_mfma_f32_16x16x32_bf16 v[232:235], v[44:47], v[200:203], v[232:235]\n"
  "ds_read_b64 v[42:43], v15 offset:36864\n"
  "s_waitcnt lgkmcnt(6)\n"
  "v_mfma_f32_16x16x32_bf16 v[240:243], v[28:31], v[200:203], v[240:243]\n"
  "ds_read_b64 v[44:45], v14 offset:40960\n"
  "v_mfma_f32_16x16x32_bf16 v[244:247], v[28:31], v[204:207], v[244:247]\n"
  "ds_read_b64 v[46:47], v15 offset:40960\n"
  "s_waitcnt lgkmcnt(6)\n"
  "v_mfma_f32_16x16x32_bf16 v[252:255], v[32:35], v[204:207], v[252:255]\n"
  "ds_read_b64 v[28:29], v14 offset:45056\n"
  "v_mfma_f32_16x16x32_bf16 v[248:251], v[32:35], v[200:203], v[248:251]\n"
  "ds_read_b64 v[30:31], v15 offset:45056\n"
  "s_waitcnt lgkmcnt(6)\n"
  "v_mfma_f32_16x16x32_bf16 v[224:227], v[36:39], v[208:211], v[224:227]\n"
  "v_xor_b32 v14, 192, v13\n"
  "v_xor_b32 v15, 224, v13\n"
  "ds_read_b64 v[32:33], v14 offset:32768\n"
  "v_mfma_f32_16x16x32_bf16 v[228:231], v[36:39], v[212:215], v[228:231]\n"
  "ds_read_b64 v[34:35], v15 offset:32768\n"
  "s_waitcnt lgkmcnt(6)\n"
  "v_mfma_f32_16x16x32_bf16 v[236:239], v[40:43], v[212:215], v[236:239]\n"
  "ds_read_b64 v[36:37], v14 offset:36864\n"
  "v_mfma_f32_16x16x32_bf16 v[232:235], v[40:43], v[208:211], v[232:235]\n"
  "ds_read_b64 v[38:39], v15 offset:36864\n"
  "s_waitcnt lgkmcnt(6)\n"
  "v_mfma_f32_16x16x32_bf16 v[240:243], v[44:47], v[208:211], v[240:243]\n"
  "ds_read_b64 v[40:41], v14 offset:40960\n"
  "v_mfma_f32_16x16x32_bf16 v[244:247], v[44:47], v[212:215], v[244:247]\n"
  "ds_read_b64 v[42:43], v15 offset:40960\n"
  "s_waitcnt lgkmcnt(6)\n"
  "v_mfma_f32_16x16x32_bf16 v[252:255], v[28:31], v[212:215], v[252:255]\n"
  "ds_read_b64 v[44:45], v14 offset:45056\n"
  "v_mfma_f32_16x16x32_bf16 v[248:251], v[28:31], v[208:211], v[248:251]\n"
  "ds_read_b64 v[46:47], v15 offset:45056\n"
  "s_waitcnt lgkmcnt(6)\n"
  "v_mfma_f32_16x16x32_bf16 v[224:227], v[32:35], v[216:219], v[224:227]\n"
  "v_mfma_f32_16x16x32_bf16 v[228:231], v[32:35], v[220:223], v[228:231]\n"
  "s_waitcnt lgkmcnt(4)\n"
  "v_mfma_f32_16x16x32_bf16 v[236:239], v[36:39], v[220:223], v[236:239]\n"
  "v_mfma_f32_16x16x32_bf16 v[232:235], v[36:39], v[216:219], v[232:235]\n"
  "s_waitcnt lgkmcnt(2)\n"
  "v_mfma_f32_16x16x32_bf16 v[240:243], v[40:43], v[216:219], v[240:243]\n"
  "v_mfma_f32_16x16x32_bf16 v[244:247], v[40:43], v[220:223], v[244:247]\n"
  "s_waitcnt lgkmcnt(0)\n"
  "v_mfma_f32_16x16x32_bf16 v[252:255], v[44:47], v[220:223], v[252:255]\n"
  "s_barrier\n"
  "v_mfma_f32_16x16x32_bf16 v[248:251], v[44:47], v[216:219], v[248:251]\n"
  "v_max3_f32 v16, v224, v225, v226\n"
  "v_max3_f32 v18, v228, v229, v230\n"
  "v_max3_f32 v16, v16, v227, v232\n"
  "v_max3_f32 v18, v18, v231, v236\n"
  "v_max3_f32 v16, v16, v233, v234\n"
  "v_max3_f32 v18, v18, v237, v238\n"
  "v_max3_f32 v16, v16, v235, v240\n"
  "v_max3_f32 v18, v18, v239, v244\n"
  "v_max3_f32 v16, v16, v241, v242\n"
  "v_max3_f32 v18, v18, v245, v246\n"
  "v_max3_f32 v16, v16, v243, v248\n"
  "v_max3_f32 v18, v18, v247, v252\n"
  "v_max3_f32 v16, v16, v249, v250\n"
  "v_max3_f32 v18, v18, v253, v254\n"
  "v_max_f32 v16, v16, v251\n"
  "v_max_f32 v18, v18, v255\n"
  "s_nop 1\n"
  "v_permlane32_swap_b32 v16, v18\n"
  "v_max_f32 v16, v16, v18\n"
  "v_mov_b32 v17, v16\n"
  "s_nop 1\n"
  "v_permlane16_swap_b32 v16, v17\n"
  "v_max_f32 v16, v16, v17\n"
  "v_mov_b32 v18, v16\n"
  "s_nop 1\n"
  "v_permlane32_swap_b32 v16, v18\n"
  "v_mul_f32 v16, s54, v16\n"
  "v_mul_f32 v18, s54, v18\n"
  "v_pk_fma_f32 v[224:225], v[224:225], s[54:55], v[16:17] op_sel_hi:[1,1,0] neg_lo:[0,0,1] neg_hi:[0,0,1]\n"
  "v_pk_fma_f32 v[228:229], v[228:229], s[54:55], v[18:19] op_sel_hi:[1,1,0] neg_lo:[0,0,1] neg_hi:[0,0,1]\n"
  "v_exp_f32 v224, v224\n"
  "v_exp_f32 v225, v225\n"
  "v_pk_fma_f32 v[226:227], v[226:227], s[54:55], v[16:17] op_sel_hi:[1,1,0] neg_lo:[0,0,1] neg_hi:[0,0,1]\n"
  "v_exp_f32 v228, v228\n"
  "v_exp_f32 v229, v229\n"
  "v_pk_fma_f32 v[230:231], v[230:231], s[54:55], v[18:19] op_sel_hi:[1,1,0] neg_lo:[0,0,1] neg_hi:[0,0,1]\n"
  "v_exp_f32 v226, v226\n"
  "v_exp_f32 v227, v227\n"
  "v_pk_fma_f32 v[232:233], v[232:233], s[54:55], v[16:17] op_sel_hi:[1,1,0] neg_lo:[0,0,1] neg_hi:[0,0,1]\n"
  "v_exp_f32 v230, v230\n"
  "v_exp_f32 v231, v231\n"
  "v_pk_add_f32 v[20:21], v[224:225], v[226:227]\n"
  "v_pk_fma_f32 v[236:237], v[236:237], s[54:55], v[18:19] op_sel_hi:[1,1,0] neg_lo:[0,0,1] neg_hi:[0,0,1]\n"
  "v_exp_f32 v232, v232\n"
  "v_exp_f32 v233, v233\n"
  "v_pk_add_f32 v[22:23], v[228:229], v[230:231]\n"
  "v_pk_fma_f32 v[234:235], v[234:235], s[54:55], v[16:17] op_sel_hi:[1,1,0] neg_lo:[0,0,1] neg_hi:[0,0,1]\n"
  "v_exp_f32 v236, v236\n"
  "v_exp_f32 v237, v237\n"
  "v_pk_add_f32 v[20:21], v[20:21], v[232:233]\n"
  "v_pk_fma_f32 v[238:239], v[238:239], s[54:55], v[18:19] op_sel_hi:[1,1,0] neg_lo:[0,0,1] neg_hi:[0,0,1]\n"
  "v_exp_f32 v234, v234\n"
  "v_exp_f32 v235, v235\n"
  "v_pk_add_f32 v[22:23], v[22:23], v[236:237]\n"
  "v_pk_fma_f32 v[240:241], v[240:241], s[54:55], v[16:17] op_sel_hi:[1,1,0] neg_lo:[0,0,1] neg_hi:[0,0,1]\n"
  "v_exp_f32 v238, v238\n"
  "v_exp_f32 v239, v239\n"
  "v_pk_add_f32 v[20:21], v[20:21], v[234:235]\n"
  "v_pk_fma_f32 v[244:245], v[244:245], s[54:55], v[18:19] op_sel_hi:[1,1,0] neg_lo:[0,0,1] neg_hi:[0,0,1]\n"
  "v_exp_f32 v240, v240\n"
  "v_exp_f32 v241, v241\n"
  "v_pk_add_f32 v[22:23], v[22:23], v[238:239]\n"
  "v_pk_fma_f32 v[242:243], v[242:243], s[54:55], v[16:17] op_sel_hi:[1,1,0] neg_lo:[0,0,1] neg_hi:[0,0,1]\n"
  "v_exp_f32 v244, v244\n"
  "v_exp_f32 v245, v245\n"
  "v_pk_add_f32 v[20:21], v[20:21], v[240:241]\n"
  "v_pk_fma_f32 v[246:247], v[246:247], s[54:55], v[18:19] op_sel_hi:[1,1,0] neg_lo:[0,0,1] neg_hi:[0,0,1]\n"
  "v_exp_f32 v242, v242\n"
  "v_exp_f32 v243, v243\n"
  "v_pk_add_f32 v[22:23], v[22:23], v[244:245]\n"
  "v_pk_fma_f32 v[248:249], v[248:249], s[54:55], v[16:17] op_sel_hi:[1,1,0] neg_lo:[0,0,1] neg_hi:[0,0,1]\n"
  "v_exp_f32 v246, v246\n"
  "v_exp_f32 v247, v247\n"
  "v_pk_add_f32 v[20:21], v[20:21], v[242:243]\n"
  "v_pk_fma_f32 v[252:253], v[252:253], s[54:55], v[18:19] op_sel_hi:[1,1,0] neg_lo:[0,0,1] neg_hi:[0,0,1]\n"
  "v_exp_f32 v248, v248\n"
  "v_exp_f32 v249, v249\n"
  "v_pk_add_f32 v[22:23], v[22:23], v[246:247]\n"
  "v_pk_fma_f32 v[250:251], v[250:251], s[54:55], v[16:17] op_sel_hi:[1,1,0] neg_lo:[0,0,1] neg_hi:[0,0,1]\n"
  "v_exp_f32 v252, v252\n"
  "v_exp_f32 v253, v253\n"
  "v_pk_add_f32 v[20:21], v[20:21], v[248:249]\n"
  "v_pk_fma_f32 v[254:255], v[254:255], s[54:55], v[18:19] op_sel_hi:[1,1,0] neg_lo:[0,0,1] neg_hi:[0,0,1]\n"
  "v_exp_f32 v250, v250\n"
  "v_exp_f32 v251, v251\n"
  "v_pk_add_f32 v[22:23], v[22:23], v[252:253]\n"
  "v_exp_f32 v254, v254\n"
  "v_exp_f32 v255, v255\n"
  "v_pk_add_f32 v[20:21], v[20:21], v[250:251]\n"
  "v_pk_add_f32 v[22:23], v[22:23], v[254:255]\n"
  "v_add_f32 v20, v20, v21\n"
  "v_add_f32 v22, v22, v23\n"
  "s_nop 1\n"
  "v_permlane32_swap_b32 v20, v22\n"
  "v_add_f32 v20, v20, v22\n"
  "v_mov_b32 v17, v20\n"
  "s_nop 1\n"
  "v_permlane16_swap_b32 v20, v17\n"
  "v_add_f32 v20, v20, v17\n"
  "v_mov_b32 v22, v20\n"
  "s_nop 1\n"
  "v_permlane32_swap_b32 v20, v22\n"
  "v_rcp_f32 v16, v20\n"
  "v_rcp_f32 v18, v22\n"
  "v_pk_mul_f32 v[224:225], v[224:225], v[16:17] op_sel_hi:[1,0]\n"
  "v_pk_mul_f32 v[226:227], v[226:227], v[16:17] op_sel_hi:[1,0]\n"
  "v_pk_mul_f32 v[232:233], v[232:233], v[16:17] op_sel_hi:[1,0]\n"
  "v_pk_mul_f32 v[234:235], v[234:235], v[16:17] op_sel_hi:[1,0]\n"
  "v_cvt_pk_bf16_f32 v192, v224, v225\n"
  "v_cvt_pk_bf16_f32 v193, v226, v227\n"
  "v_cvt_pk_bf16_f32 v194, v232, v233\n"
  "v_cvt_pk_bf16_f32 v195, v234, v235\n"
  "v_pk_mul_f32 v[228:229], v[228:229], v[18:19] op_sel_hi:[1,0]\n"
  "v_pk_mul_f32 v[230:231], v[230:231], v[18:19] op_sel_hi:[1,0]\n"
  "s_barrier\n"
  "v_add_u32 v6, 131072, v6\n"
  "v_add_u32 v7, 131072, v7\n"
  "v_add_u32 v8, 131072, v8\n"
  "v_add_u32 v9, 131072, v9\n"
  "v_add_u32 v6, s38, v6\n"
  "v_add_u32 v7, s38, v7\n"
  "v_add_u32 v8, s38, v8\n"
  "v_add_u32 v9, s38, v9\n"
  "ds_read_b64_tr_b16 v[28:29], v6 offset:0\n"
  "ds_read_b64_tr_b16 v[30:31], v6 offset:2048\n"
  "ds_read_b64_tr_b16 v[32:33], v7 offset:0\n"
  "ds_read_b64_tr_b16 v[34:35], v7 offset:2048\n"
  "ds_read_b64_tr_b16 v[36:37], v8 offset:0\n"
  "ds_read_b64_tr_b16 v[38:39], v8 offset:2048\n"
  "ds_read_b64_tr_b16 v[40:41], v9 offset:0\n"
  "ds_read_b64_tr_b16 v[42:43], v9 offset:2048\n"
  "v_pk_mul_f32 v[236:237], v[236:237], v[18:19] op_sel_hi:[1,0]\n"
  "v_pk_mul_f32 v[238:239], v[238:239], v[18:19] op_sel_hi:[1,0]\n"
  "v_cvt_pk_bf16_f32 v196, v228, v229\n"
  "v_cvt_pk_bf16_f32 v197, v230, v231\n"
  "v_cvt_pk_bf16_f32 v198, v236, v237\n"
  "v_cvt_pk_bf16_f32 v199, v238, v239\n"
  "v_pk_mul_f32 v[240:241], v[240:241], v[16:17] op_sel_hi:[1,0]\n"
  "v_pk_mul_f32 v[242:243], v[242:243], v[16:17] op_sel_hi:[1,0]\n"
  "v_pk_mul_f32 v[248:249], v[248:249], v[16:17] op_sel_hi:[1,0]\n"
  "v_pk_mul_f32 v[250:251], v[250:251], v[16:17] op_sel_hi:[1,0]\n"
  "v_cvt_pk_bf16_f32 v200, v240, v241\n"
  "v_cvt_pk_bf16_f32 v201, v242, v243\n"
  "v_cvt_pk_bf16_f32 v202, v248, v249\n"
  "v_cvt_pk_bf16_f32 v203, v250, v251\n"
  "v_pk_mul_f32 v[244:245], v[244:245], v[18:19] op_sel_hi:[1,0]\n"
  "v_pk_mul_f32 v[246:247], v[246:247], v[18:19] op_sel_hi:[1,0]\n"
  "v_pk_mul_f32 v[252:253], v[252:253], v[18:19] op_sel_hi:[1,0]\n"
  "v_pk_mul_f32 v[254:255], v[254:255], v[18:19] op_sel_hi:[1,0]\n"
  "v_cvt_pk_bf16_f32 v204, v244, v245\n"
  "v_cvt_pk_bf16_f32 v205, v246, v247\n"
  "v_cvt_pk_bf16_f32 v206, v252, v253\n"
  "v_cvt_pk_bf16_f32 v207, v254, v255\n"
  "s_waitcnt lgkmcnt(6)\n"
  "v_mfma_f32_16x16x32_bf16 v[64:67], v[28:31], v[192:195], 0\n"
  "ds_read_b64_tr_b16 v[44:45], v6 offset:8192\n"
  "v_mfma_f32_16x16x32_bf16 v[68:71], v[28:31], v[196:199], 0\n"
  "ds_read_b64_tr_b16 v[46:47], v6 offset:10240\n"
  "s_waitcnt lgkmcnt(6)\n"
  "v_mfma_f32_16x16x32_bf16 v[76:79], v[32:35], v[196:199], 0\n"
  "ds_read_b64_tr_b16 v[28:29], v7 offset:8192\n"
  "v_mfma_f32_16x16x32_bf16 v[72:75], v[32:35], v[192:195], 0\n"
  "ds_read_b64_tr_b16 v[30:31], v7 offset:10240\n"
  "s_waitcnt lgkmcnt(6)\n"
  "v_mfma_f32_16x16x32_bf16 v[80:83], v[36:39], v[192:195], 0\n"
  "ds_read_b64_tr_b16 v[32:33], v8 offset:8192\n"
  "v_mfma_f32_16x16x32_bf16 v[84:87], v[36:39], v[196:199], 0\n"
  "ds_read_b64_tr_b16 v[34:35], v8 offset:10240\n"
  "s_waitcnt lgkmcnt(6)\n"
  "v_mfma_f32_16x16x32_bf16 v[92:95], v[40:43], v[196:199], 0\n"
  "ds_read_b64_tr_b16 v[36:37], v9 offset:8192\n"
  "v_mfma_f32_16x16x32_bf16 v[88:91], v[40:43], v[192:195], 0\n"
  "ds_read_b64_tr_b16 v[38:39], v9 offset:10240\n"
  "s_waitcnt lgkmcnt(6)\n"
  "v_mfma_f32_16x16x32_bf16 v[96:99], v[44:47], v[192:195], 0\n"
  "ds_read_b64_tr_b16 v[40:41], v6 offset:4096\n"
  "v_mfma_f32_16x16x32_bf16 v[100:103], v[44:47], v[196:199], 0\n"
  "ds_read_b64_tr_b16 v[42:43], v6 offset:6144\n"
  "s_waitcnt lgkmcnt(6)\n"
  "v_mfma_f32_16x16x32_bf16 v[108:111], v[28:31], v[196:199], 0\n"
  "ds_read_b64_tr_b16 v[44:45], v7 offset:4096\n"
  "v_mfma_f32_16x16x32_bf16 v[104:107], v[28:31], v[192:195], 0\n"
  "ds_read_b64_tr_b16 v[46:47], v7 offset:6144\n"
  "s_waitcnt lgkmcnt(6)\n"
  "v_mfma_f32_16x16x32_bf16 v[112:115], v[32:35], v[192:195], 0\n"
  "ds_read_b64_tr_b16 v[28:29], v8 offset:4096\n"
  "v_mfma_f32_16x16x32_bf16 v[116:119], v[32:35], v[196:199], 0\n"
  "ds_read_b64_tr_b16 v[30:31], v8 offset:6144\n"
  "s_waitcnt lgkmcnt(6)\n"
  "v_mfma_f32_16x16x32_bf16 v[124:127], v[36:39], v[196:199], 0\n"
  "ds_read_b64_tr_b16 v[32:33], v9 offset:4096\n"
  "v_mfma_f32_16x16x32_bf16 v[120:123], v[36:39], v[192:195], 0\n"
  "ds_read_b64_tr_b16 v[34:35], v9 offset:6144\n"
  "s_waitcnt lgkmcnt(6)\n"
  "v_mfma_f32_16x16x32_bf16 v[64:67], v[40:43], v[200:203], v[64:67]\n"
  "ds_read_b64_tr_b16 v[36:37], v6 offset:12288\n"
  "v_mfma_f32_16x16x32_bf16 v[68:71], v[40:43], v[204:207], v[68:71]\n"
  "ds_read_b64_tr_b16 v[38:39], v6 offset:14336\n"
  "s_waitcnt lgkmcnt(6)\n"
  "v_mfma_f32_16x16x32_bf16 v[76:79], v[44:47], v[204:207], v[76:79]\n"
  "ds_read_b64_tr_b16 v[40:41], v7 offset:12288\n"
  "v_mfma_f32_16x16x32_bf16 v[72:75], v[44:47], v[200:203], v[72:75]\n"
  "ds_read_b64_tr_b16 v[42:43], v7 offset:14336\n"
  "s_waitcnt lgkmcnt(6)\n"
  "v_mfma_f32_16x16x32_bf16 v[80:83], v[28:31], v[200:203], v[80:83]\n"
  "ds_read_b64_tr_b16 v[44:45], v8 offset:12288\n"
  "v_mfma_f32_16x16x32_bf16 v[84:87], v[28:31], v[204:207], v[84:87]\n"
  "ds_read_b64_tr_b16 v[46:47], v8 offset:14336\n"
  "s_waitcnt lgkmcnt(6)\n"
  "v_mfma_f32_16x16x32_bf16 v[92:95], v[32:35], v[204:207], v[92:95]\n"
  "ds_read_b64_tr_b16 v[28:29], v9 offset:12288\n"
  "v_mfma_f32_16x16x32_bf16 v[88:91], v[32:35], v[200:203], v[88:91]\n"
  "ds_read_b64_tr_b16 v[30:31], v9 offset:14336\n"
  "s_waitcnt lgkmcnt(6)\n"
  "v_mfma_f32_16x16x32_bf16 v[96:99], v[36:39], v[200:203], v[96:99]\n"
  "s_barrier\n"
  "ds_read_b64_tr_b16 v[32:33], v2 offset:32768\n"
  "v_mfma_f32_16x16x32_bf16 v[100:103], v[36:39], v[204:207], v[100:103]\n"
  "ds_read_b64_tr_b16 v[34:35], v2 offset:33280\n"
  "ds_read_b128 v[48:51], v10 offset:0\n"
  "ds_read_b128 v[52:55], v10 offset:2048\n"
  "s_waitcnt lgkmcnt(8)\n"
  "v_mfma_f32_16x16x32_bf16 v[108:111], v[40:43], v[204:207], v[108:111]\n"
  "ds_read_b64_tr_b16 v[36:37], v3 offset:32768\n"
  "v_mfma_f32_16x16x32_bf16 v[104:107], v[40:43], v[200:203], v[104:107]\n"
  "ds_read_b64_tr_b16 v[38:39], v3 offset:33280\n"
  "s_waitcnt lgkmcnt(8)\n"
  "v_mfma_f32_16x16x32_bf16 v[112:115], v[44:47], v[200:203], v[112:115]\n"
  "ds_read_b64_tr_b16 v[40:41], v4 offset:32768\n"
  "v_mfma_f32_16x16x32_bf16 v[116:119], v[44:47], v[204:207], v[116:119]\n"
  "ds_read_b64_tr_b16 v[42:43], v4 offset:33280\n"
  "s_waitcnt lgkmcnt(8)\n"
  "v_mfma_f32_16x16x32_bf16 v[124:127], v[28:31], v[204:207], v[124:127]\n"
  "ds_read_b64_tr_b16 v[44:45], v5 offset:32768\n"
  "v_mfma_f32_16x16x32_bf16 v[120:123], v[28:31], v[200:203], v[120:123]\n"
  "ds_read_b64_tr_b16 v[46:47], v5 offset:33280\n"
  "v_cvt_pk_bf16_f32 v64, v64, v65\n"
  "v_cvt_pk_bf16_f32 v65, v66, v67\n"
  "v_cvt_pk_bf16_f32 v66, v72, v73\n"
  "v_cvt_pk_bf16_f32 v67, v74, v75\n"
  "s_waitcnt lgkmcnt(7)\n"
  "v_mfma_f32_16x16x32_bf16 v[128:131], v[32:35], v[48:51], v[128:131]\n"
  "ds_read_b64_tr_b16 v[28:29], v2 offset:40960\n"
  "s_waitcnt lgkmcnt(7)\n"
  "v_mfma_f32_16x16x32_bf16 v[132:135], v[32:35], v[52:55], v[132:135]\n"
  "ds_read_b64_tr_b16 v[30:31], v2 offset:41472\n"
  "ds_read_b128 v[56:59], v11 offset:0\n"
  "ds_read_b128 v[60:63], v11 offset:2048\n"
  "s_add_u32 s39, s67, 0\n"
  "buffer_store_dwordx4 v[64:67], v1, s[24:27], s39 offen sc1\n"
  "v_cvt_pk_bf16_f32 v68, v68, v69\n"
  "v_cvt_pk_bf16_f32 v69, v70, v71\n"
  "v_cvt_pk_bf16_f32 v70, v76, v77\n"
  "s_waitcnt lgkmcnt(8)\n"
  "v_mfma_f32_16x16x32_bf16 v[140:143], v[36:39], v[52:55], v[140:143]\n"
  "ds_read_b64_tr_b16 v[32:33], v3 offset:40960\n"
  "v_mfma_f32_16x16x32_bf16 v[136:139], v[36:39], v[48:51], v[136:139]\n"
  "ds_read_b64_tr_b16 v[34:35], v3 offset:41472\n"
  "v_cvt_pk_bf16_f32 v71, v78, v79\n"
  "buffer_store_dwordx4 v[68:71], v1, s[24:27], s39 offen offset:1024 sc1\n"
  "v_cvt_pk_bf16_f32 v72, v80, v81\n"
  "v_cvt_pk_bf16_f32 v73, v82, v83\n"
  "s_waitcnt lgkmcnt(8)\n"
  "v_mfma_f32_16x16x32_bf16 v[144:147], v[40:43], v[48:51], v[144:147]\n"
  "ds_read_b64_tr_b16 v[36:37], v4 offset:40960\n"
  "v_mfma_f32_16x16x32_bf16 v[148:151], v[40:43], v[52:55], v[148:151]\n"
  "ds_read_b64_tr_b16 v[38:39], v4 offset:41472\n"
  "v_cvt_pk_bf16_f32 v74, v88, v89\n"
  "v_cvt_pk_bf16_f32 v75, v90, v91\n"
  "buffer_store_dwordx4 v[72:75], v1, s[24:27], s39 offen offset:2048 sc1\n"
  "v_cvt_pk_bf16_f32 v76, v84, v85\n"
  "s_waitcnt lgkmcnt(8)\n"
  "v_mfma_f32_16x16x32_bf16 v[156:159], v[44:47], v[52:55], v[156:159]\n"
  "ds_read_b64_tr_b16 v[40:41], v5 offset:40960\n"
  "v_mfma_f32_16x16x32_bf16 v[152:155], v[44:47], v[48:51], v[152:155]\n"
  "ds_read_b64_tr_b16 v[42:43], v5 offset:41472\n"
  "v_cvt_pk_bf16_f32 v77, v86, v87\n"
  "v_cvt_pk_bf16_f32 v78, v92, v93\n"
  "v_cvt_pk_bf16_f32 v79, v94, v95\n"
  "buffer_store_dwordx4 v[76:79], v1, s[24:27], s39 offen offset:3072 sc1\n"
  "s_waitcnt lgkmcnt(8)\n"
  "v_mfma_f32_16x16x32_bf16 v[160:163], v[28:31], v[48:51], v[160:163]\n"
  "ds_read_b64_tr_b16 v[44:45], v2 offset:36864\n"
  "v_mfma_f32_16x16x32_bf16 v[164:167], v[28:31], v[52:55], v[164:167]\n"
  "ds_read_b64_tr_b16 v[46:47], v2 offset:37376\n"
  "v_cvt_pk_bf16_f32 v80, v96, v97\n"
  "v_cvt_pk_bf16_f32 v81, v98, v99\n"
  "v_cvt_pk_bf16_f32 v82, v104, v105\n"
  "v_cvt_pk_bf16_f32 v83, v106, v107\n"
  "s_waitcnt lgkmcnt(6)\n"
  "v_mfma_f32_16x16x32_bf16 v[172:175], v[32:35], v[52:55], v[172:175]\n"
  "ds_read_b64_tr_b16 v[28:29], v3 offset:36864\n"
  "v_mfma_f32_16x16x32_bf16 v[168:171], v[32:35], v[48:51], v[168:171]\n"
  "ds_read_b64_tr_b16 v[30:31], v3 offset:37376\n"
  "s_add_u32 s39, s67, 4096\n"
  "buffer_store_dwordx4 v[80:83], v1, s[24:27], s39 offen sc1\n"
  "v_cvt_pk_bf16_f32 v84, v100, v101\n"
  "v_cvt_pk_bf16_f32 v85, v102, v103\n"
  "v_cvt_pk_bf16_f32 v86, v108, v109\n"
  "s_waitcnt lgkmcnt(6)\n"
  "v_mfma_f32_16x16x32_bf16 v[176:179], v[36:39], v[48:51], v[176:179]\n"
  "ds_read_b64_tr_b16 v[32:33], v4 offset:36864\n"
  "v_mfma_f32_16x16x32_bf16 v[180:183], v[36:39], v[52:55], v[180:183]\n"
  "ds_read_b64_tr_b16 v[34:35], v4 offset:37376\n"
  "v_cvt_pk_bf16_f32 v87, v110, v111\n"
  "buffer_store_dwordx4 v[84:87], v1, s[24:27], s39 offen offset:1024 sc1\n"
  "v_cvt_pk_bf16_f32 v88, v112, v113\n"
  "v_cvt_pk_bf16_f32 v89, v114, v115\n"
  "s_waitcnt lgkmcnt(6)\n"
  "v_mfma_f32_16x16x32_bf16 v[188:191], v[40:43], v[52:55], v[188:191]\n"
  "ds_read_b64_tr_b16 v[36:37], v5 offset:36864\n"
  "v_mfma_f32_16x16x32_bf16 v[184:187], v[40:43], v[48:51], v[184:187]\n"
  "ds_read_b64_tr_b16 v[38:39], v5 offset:37376\n"
  "v_cvt_pk_bf16_f32 v90, v120, v121\n"
  "v_cvt_pk_bf16_f32 v91, v122, v123\n"
  "buffer_store_dwordx4 v[88:91], v1, s[24:27], s39 offen offset:2048 sc1\n"
  "v_cvt_pk_bf16_f32 v92, v116, v117\n"
  "s_waitcnt lgkmcnt(6)\n"
  "v_mfma_f32_16x16x32_bf16 v[128:131], v[44:47], v[56:59], v[128:131]\n"
  "ds_read_b64_tr_b16 v[40:41], v2 offset:45056\n"
  "v_mfma_f32_16x16x32_bf16 v[132:135], v[44:47], v[60:63], v[132:135]\n"
  "ds_read_b64_tr_b16 v[42:43], v2 offset:45568\n"
  "v_cvt_pk_bf16_f32 v93, v118, v119\n"
  "v_cvt_pk_bf16_f32 v94, v124, v125\n"
  "v_cvt_pk_bf16_f32 v95, v126, v127\n"
  "buffer_store_dwordx4 v[92:95], v1, s[24:27], s39 offen offset:3072 sc1\n"
  "s_waitcnt lgkmcnt(6)\n"
  "v_mfma_f32_16x16x32_bf16 v[140:143], v[28:31], v[60:63], v[140:143]\n"
  "ds_read_b64_tr_b16 v[44:45], v3 offset:45056\n"
  "v_mfma_f32_16x16x32_bf16 v[136:139], v[28:31], v[56:59], v[136:139]\n"
  "ds_read_b64_tr_b16 v[46:47], v3 offset:45568\n"
  "s_waitcnt lgkmcnt(6)\n"
  "v_mfma_f32_16x16x32_bf16 v[144:147], v[32:35], v[56:59], v[144:147]\n"
  "ds_read_b64_tr_b16 v[28:29], v4 offset:45056\n"
  "v_mfma_f32_16x16x32_bf16 v[148:151], v[32:35], v[60:63], v[148:151]\n"
  "ds_read_b64_tr_b16 v[30:31], v4 offset:45568\n"
  "s_waitcnt lgkmcnt(6)\n"
  "v_mfma_f32_16x16x32_bf16 v[156:159], v[36:39], v[60:63], v[156:159]\n"
  "ds_read_b64_tr_b16 v[32:33], v5 offset:45056\n"
  "v_mfma_f32_16x16x32_bf16 v[152:155], v[36:39], v[56:59], v[152:155]\n"
  "ds_read_b64_tr_b16 v[34:35], v5 offset:45568\n"
  "s_waitcnt lgkmcnt(6)\n"
  "v_mfma_f32_16x16x32_bf16 v[160:163], v[40:43], v[56:59], v[160:163]\n"
  "s_barrier\n"
  "ds_read_b64_tr_b16 v[36:37], v2 offset:49152\n"
  "v_mfma_f32_16x16x32_bf16 v[164:167], v[40:43], v[60:63], v[164:167]\n"
  "ds_read_b64_tr_b16 v[38:39], v2 offset:49664\n"
  "ds_read_b128 v[48:51], v10 offset:16384\n"
  "ds_read_b128 v[52:55], v10 offset:18432\n"
  "s_waitcnt lgkmcnt(8)\n"
  "v_mfma_f32_16x16x32_bf16 v[172:175], v[44:47], v[60:63], v[172:175]\n"
  "ds_read_b64_tr_b16 v[40:41], v3 offset:49152\n"
  "v_mfma_f32_16x16x32_bf16 v[168:171], v[44:47], v[56:59], v[168:171]\n"
  "ds_read_b64_tr_b16 v[42:43], v3 offset:49664\n"
  "s_waitcnt lgkmcnt(8)\n"
  "v_mfma_f32_16x16x32_bf16 v[176:179], v[28:31], v[56:59], v[176:179]\n"
  "ds_read_b64_tr_b16 v[44:45], v4 offset:49152\n"
  "v_mfma_f32_16x16x32_bf16 v[180:183], v[28:31], v[60:63], v[180:183]\n"
  "ds_read_b64_tr_b16 v[46:47], v4 offset:49664\n"
  "s_waitcnt lgkmcnt(8)\n"
  "v_mfma_f32_16x16x32_bf16 v[188:191], v[32:35], v[60:63], v[188:191]\n"
  "ds_read_b64_tr_b16 v[28:29], v5 offset:49152\n"
  "v_mfma_f32_16x16x32_bf16 v[184:187], v[32:35], v[56:59], v[184:187]\n"
  "ds_read_b64_tr_b16 v[30:31], v5 offset:49664\n"
  "s_waitcnt lgkmcnt(7)\n"
  "v_mfma_f32_16x16x32_bf16 v[128:131], v[36:39], v[48:51], v[128:131]\n"
  "ds_read_b64_tr_b16 v[32:33], v2 offset:57344\n"
  "s_waitcnt lgkmcnt(7)\n"
  "v_mfma_f32_16x16x32_bf16 v[132:135], v[36:39], v[52:55], v[132:135]\n"
  "ds_read_b64_tr_b16 v[34:35], v2 offset:57856\n"
  "ds_read_b128 v[56:59], v11 offset:16384\n"
  "ds_read_b128 v[60:63], v11 offset:18432\n"
  "s_waitcnt lgkmcnt(8)\n"
  "v_mfma_f32_16x16x32_bf16 v[140:143], v[40:43], v[52:55], v[140:143]\n"
  "ds_read_b64_tr_b16 v[36:37], v3 offset:57344\n"
  "v_mfma_f32_16x16x32_bf16 v[136:139], v[40:43], v[48:51], v[136:139]\n"
  "ds_read_b64_tr_b16 v[38:39], v3 offset:57856\n"
  "s_waitcnt lgkmcnt(8)\n"
  "v_mfma_f32_16x16x32_bf16 v[144:147], v[44:47], v[48:51], v[144:147]\n"
  "ds_read_b64_tr_b16 v[40:41], v4 offset:57344\n"
  "v_mfma_f32_16x16x32_bf16 v[148:151], v[44:47], v[52:55], v[148:151]\n"
  "ds_read_b64_tr_b16 v[42:43], v4 offset:57856\n"
  "s_waitcnt lgkmcnt(8)\n"
  "v_mfma_f32_16x16x32_bf16 v[156:159], v[28:31], v[52:55], v[156:159]\n"
  "ds_read_b64_tr_b16 v[44:45], v5 offset:57344\n"
  "v_mfma_f32_16x16x32_bf16 v[152:155], v[28:31], v[48:51], v[152:155]\n"
  "ds_read_b64_tr_b16 v[46:47], v5 offset:57856\n"
  "s_waitcnt lgkmcnt(8)\n"
  "v_mfma_f32_16x16x32_bf16 v[160:163], v[32:35], v[48:51], v[160:163]\n"
  "ds_read_b64_tr_b16 v[28:29], v2 offset:53248\n"
  "v_mfma_f32_16x16x32_bf16 v[164:167], v[32:35], v[52:55], v[164:167]\n"
  "ds_read_b64_tr_b16 v[30:31], v2 offset:53760\n"
  "s_waitcnt lgkmcnt(6)\n"
  "v_mfma_f32_16x16x32_bf16 v[172:175], v[36:39], v[52:55], v[172:175]\n"
  "ds_read_b64_tr_b16 v[32:33], v3 offset:53248\n"
  "v_mfma_f32_16x16x32_bf16 v[168:171], v[36:39], v[48:51], v[168:171]\n"
  "ds_read_b64_tr_b16 v[34:35], v3 offset:53760\n"
  "s_waitcnt lgkmcnt(6)\n"
  "v_mfma_f32_16x16x32_bf16 v[176:179], v[40:43], v[48:51], v[176:179]\n"
  "ds_read_b64_tr_b16 v[36:37], v4 offset:53248\n"
  "v_mfma_f32_16x16x32_bf16 v[180:183], v[40:43], v[52:55], v[180:183]\n"
  "ds_read_b64_tr_b16 v[38:39], v4 offset:53760\n"
  "s_waitcnt lgkmcnt(6)\n"
  "v_mfma_f32_16x16x32_bf16 v[188:191], v[44:47], v[52:55], v[188:191]\n"
  "ds_read_b64_tr_b16 v[40:41], v5 offset:53248\n"
  "v_mfma_f32_16x16x32_bf16 v[184:187], v[44:47], v[48:51], v[184:187]\n"
  "ds_read_b64_tr_b16 v[42:43], v5 offset:53760\n"
  "s_waitcnt lgkmcnt(6)\n"
  "v_mfma_f32_16x16x32_bf16 v[128:131], v[28:31], v[56:59], v[128:131]\n"
  "ds_read_b64_tr_b16 v[44:45], v2 offset:61440\n"
  "v_mfma_f32_16x16x32_bf16 v[132:135], v[28:31], v[60:63], v[132:135]\n"
  "ds_read_b64_tr_b16 v[46:47], v2 offset:61952\n"
  "s_waitcnt lgkmcnt(6)\n"
  "v_mfma_f32_16x16x32_bf16 v[140:143], v[32:35], v[60:63], v[140:143]\n"
  "ds_read_b64_tr_b16 v[28:29], v3 offset:61440\n"
  "v_mfma_f32_16x16x32_bf16 v[136:139], v[32:35], v[56:59], v[136:139]\n"
  "ds_read_b64_tr_b16 v[30:31], v3 offset:61952\n"
  "s_waitcnt lgkmcnt(6)\n"
  "v_mfma_f32_16x16x32_bf16 v[144:147], v[36:39], v[56:59], v[144:147]\n"
  "ds_read_b64_tr_b16 v[32:33], v4 offset:61440\n"
  "v_mfma_f32_16x16x32_bf16 v[148:151], v[36:39], v[60:63], v[148:151]\n"
  "ds_read_b64_tr_b16 v[34:35], v4 offset:61952\n"
  "s_waitcnt lgkmcnt(6)\n"
  "v_mfma_f32_16x16x32_bf16 v[156:159], v[40:43], v[60:63], v[156:159]\n"
  "ds_read_b64_tr_b16 v[36:37], v5 offset:61440\n"
  "v_mfma_f32_16x16x32_bf16 v[152:155], v[40:43], v[56:59], v[152:155]\n"
  "ds_read_b64_tr_b16 v[38:39], v5 offset:61952\n"
  "v_cvt_pk_bf16_f32 v192, v128, v129\n"
  "v_cvt_pk_bf16_f32 v193, v130, v131\n"
  "v_cvt_pk_bf16_f32 v194, v136, v137\n"
  "v_cvt_pk_bf16_f32 v195, v138, v139\n"
  "s_waitcnt lgkmcnt(6)\n"
  "v_mfma_f32_16x16x32_bf16 v[160:163], v[44:47], v[56:59], v[160:163]\n"
  "s_barrier\n"
  "v_add_u32 v13, 4294901760, v13\n"
  "v_xor_b32 v14, 0, v13\n"
  "v_xor_b32 v15, 32, v13\n"
  "ds_read_b64 v[40:41], v14 offset:0\n"
  "v_mfma_f32_16x16x32_bf16 v[164:167], v[44:47], v[60:63], v[164:167]\n"
  "ds_read_b64 v[42:43], v15 offset:0\n"
  "v_cvt_pk_bf16_f32 v196, v132, v133\n"
  "v_cvt_pk_bf16_f32 v197, v134, v135\n"
  "v_cvt_pk_bf16_f32 v198, v140, v141\n"
  "v_cvt_pk_bf16_f32 v199, v142, v143\n"
  "s_waitcnt lgkmcnt(6)\n"
  "v_mfma_f32_16x16x32_bf16 v[172:175], v[28:31], v[60:63], v[172:175]\n"
  "ds_read_b64 v[44:45], v14 offset:4096\n"
  "v_mfma_f32_16x16x32_bf16 v[168:171], v[28:31], v[56:59], v[168:171]\n"
  "ds_read_b64 v[46:47], v15 offset:4096\n"
  "v_cvt_pk_bf16_f32 v200, v144, v145\n"
  "v_cvt_pk_bf16_f32 v201, v146, v147\n"
  "v_cvt_pk_bf16_f32 v202, v152, v153\n"
  "v_cvt_pk_bf16_f32 v203, v154, v155\n"
  "s_waitcnt lgkmcnt(6)\n"
  "v_mfma_f32_16x16x32_bf16 v[176:179], v[32:35], v[56:59], v[176:179]\n"
  "ds_read_b64 v[28:29], v14 offset:8192\n"
  "v_mfma_f32_16x16x32_bf16 v[180:183], v[32:35], v[60:63], v[180:183]\n"
  "ds_read_b64 v[30:31], v15 offset:8192\n"
  "v_cvt_pk_bf16_f32 v204, v148, v149\n"
  "v_cvt_pk_bf16_f32 v205, v150, v151\n"
  "v_cvt_pk_bf16_f32 v206, v156, v157\n"
  "v_cvt_pk_bf16_f32 v207, v158, v159\n"
  "s_mov_b64 exec, 1\n"
  "s_waitcnt vmcnt(0)\n"
  "buffer_store_dwordx2 v[26:27], v24, s[24:27], s69 offen sc1\n"
  "s_mov_b64 exec, -1\n"
  "s_waitcnt lgkmcnt(6)\n"
  "v_mfma_f32_16x16x32_bf16 v[188:191], v[36:39], v[60:63], v[188:191]\n"
  "ds_read_b64 v[32:33], v14 offset:12288\n"
  "v_mfma_f32_16x16x32_bf16 v[184:187], v[36:39], v[56:59], v[184:187]\n"
  "ds_read_b64 v[34:35], v15 offset:12288\n"
  "v_cvt_pk_bf16_f32 v208, v160, v161\n"
  "v_cvt_pk_bf16_f32 v209, v162, v163\n"
  "v_cvt_pk_bf16_f32 v210, v168, v169\n"
  "v_cvt_pk_bf16_f32 v211, v170, v171\n"
  "v_cvt_pk_bf16_f32 v212, v164, v165\n"
  "v_cvt_pk_bf16_f32 v213, v166, v167\n"
  "v_cvt_pk_bf16_f32 v214, v172, v173\n"
  "v_cvt_pk_bf16_f32 v215, v174, v175\n"
  "v_cvt_pk_bf16_f32 v216, v176, v177\n"
  "v_cvt_pk_bf16_f32 v217, v178, v179\n"
  "v_cvt_pk_bf16_f32 v218, v184, v185\n"
  "v_cvt_pk_bf16_f32 v219, v186, v187\n"
  "v_cvt_pk_bf16_f32 v220, v180, v181\n"
  "v_cvt_pk_bf16_f32 v221, v182, v183\n"
  "v_cvt_pk_bf16_f32 v222, v188, v189\n"
  "v_cvt_pk_bf16_f32 v223, v190, v191\n"
  "s_waitcnt lgkmcnt(6)\n"
  "v_mfma_f32_16x16x32_bf16 v[224:227], v[40:43], v[192:195], 0\n"
  "v_xor_b32 v14, 64, v13\n"
  "v_xor_b32 v15, 96, v13\n"
  "ds_read_b64 v[36:37], v14 offset:0\n"
  "v_mfma_f32_16x16x32_bf16 v[228:231], v[40:43], v[196:199], 0\n"
  "ds_read_b64 v[38:39], v15 offset:0\n"
  "s_waitcnt lgkmcnt(6)\n"
  "v_mfma_f32_16x16x32_bf16 v[236:239], v[44:47], v[196:199], 0\n"
  "ds_read_b64 v[40:41], v14 offset:4096\n"
  "v_mfma_f32_16x16x32_bf16 v[232:235], v[44:47], v[192:195], 0\n"
  "ds_read_b64 v[42:43], v15 offset:4096\n"
  "s_waitcnt lgkmcnt(6)\n"
  "v_mfma_f32_16x16x32_bf16 v[240:243], v[28:31], v[192:195], 0\n"
  "ds_read_b64 v[44:45], v14 offset:8192\n"
  "v_mfma_f32_16x16x32_bf16 v[244:247], v[28:31], v[196:199], 0\n"
  "ds_read_b64 v[46:47], v15 offset:8192\n"
  "s_waitcnt lgkmcnt(6)\n"
  "v_mfma_f32_16x16x32_bf16 v[252:255], v[32:35], v[196:199], 0\n"
  "s_barrier\n"
  "ds_read_b64 v[28:29], v14 offset:12288\n"
  "v_mfma_f32_16x16x32_bf16 v[248:251], v[32:35], v[192:195], 0\n"
  "ds_read_b64 v[30:31], v15 offset:12288\n"
  "s_waitcnt lgkmcnt(6)\n"
  "v_mfma_f32_16x16x32_bf16 v[224:227], v[36:39], v[200:203], v[224:227]\n"
  "v_xor_b32 v14, 128, v13\n"
  "v_xor_b32 v15, 160, v13\n"
  "ds_read_b64 v[32:33], v14 offset:0\n"
  "v_mfma_f32_16x16x32_bf16 v[228:231], v[36:39], v[204:207], v[228:231]\n"
  "ds_read_b64 v[34:35], v15 offset:0\n"
  "s_waitcnt lgkmcnt(6)\n"
  "v_mfma_f32_16x16x32_bf16 v[236:239], v[40:43], v[204:207], v[236:239]\n"
  "ds_read_b64 v[36:37], v14 offset:4096\n"
  "v_mfma_f32_16x16x32_bf16 v[232:235], v[40:43], v[200:203], v[232:235]\n"
  "ds_read_b64 v[38:39], v15 offset:4096\n"
  "s_waitcnt lgkmcnt(6)\n"
  "v_mfma_f32_16x16x32_bf16 v[240:243], v[44:47], v[200:203], v[240:243]\n"
  "ds_read_b64 v[40:41], v14 offset:8192\n"
  "v_mfma_f32_16x16x32_bf16 v[244:247], v[44:47], v[204:207], v[244:247]\n"
  "ds_read_b64 v[42:43], v15 offset:8192\n"
  "s_waitcnt lgkmcnt(6)\n"
  "v_mfma_f32_16x16x32_bf16 v[252:255], v[28:31], v[204:207], v[252:255]\n"
  "ds_read_b64 v[44:45], v14 offset:12288\n"
  "v_mfma_f32_16x16x32_bf16 v[248:251], v[28:31], v[200:203], v[248:251]\n"
  "ds_read_b64 v[46:47], v15 offset:12288\n"
  "s_waitcnt lgkmcnt(6)\n"
  "v_mfma_f32_16x16x32_bf16 v[224:227], v[32:35], v[208:211], v[224:227]\n"
  "v_xor_b32 v14, 192, v13\n"
  "v_xor_b32 v15, 224, v13\n"
  "ds_read_b64 v[28:29], v14 offset:0\n"
  "v_mfma_f32_16x16x32_bf16 v[228:231], v[32:35], v[212:215], v[228:231]\n"
  "ds_read_b64 v[30:31], v15 offset:0\n"
  "s_waitcnt lgkmcnt(6)\n"
  "v_mfma_f32_16x16x32_bf16 v[236:239], v[36:39], v[212:215], v[236:239]\n"
  "ds_read_b64 v[32:33], v14 offset:4096\n"
  "v_mfma_f32_16x16x32_bf16 v[232:235], v[36:39], v[208:211], v[232:235]\n"
  "ds_read_b64 v[34:35], v15 offset:4096\n"
  "s_waitcnt lgkmcnt(6)\n"
  "v_mfma_f32_16x16x32_bf16 v[240:243], v[40:43], v[208:211], v[240:243]\n"
  "ds_read_b64 v[36:37], v14 offset:8192\n"
  "v_mfma_f32_16x16x32_bf16 v[244:247], v[40:43], v[212:215], v[244:247]\n"
  "ds_read_b64 v[38:39], v15 offset:8192\n"
  "s_waitcnt lgkmcnt(6)\n"
  "v_mfma_f32_16x16x32_bf16 v[252:255], v[44:47], v[212:215], v[252:255]\n"
  "ds_read_b64 v[40:41], v14 offset:12288\n"
  "v_mfma_f32_16x16x32_bf16 v[248:251], v[44:47], v[208:211], v[248:251]\n"
  "ds_read_b64 v[42:43], v15 offset:12288\n"
  "s_waitcnt lgkmcnt(6)\n"
  "v_mfma_f32_16x16x32_bf16 v[224:227], v[28:31], v[216:219], v[224:227]\n"
  "v_mfma_f32_16x16x32_bf16 v[228:231], v[28:31], v[220:223], v[228:231]\n"
  "s_waitcnt lgkmcnt(4)\n"
  "v_mfma_f32_16x16x32_bf16 v[236:239], v[32:35], v[220:223], v[236:239]\n"
  "v_mfma_f32_16x16x32_bf16 v[232:235], v[32:35], v[216:219], v[232:235]\n"
  "s_waitcnt lgkmcnt(2)\n"
  "v_mfma_f32_16x16x32_bf16 v[240:243], v[36:39], v[216:219], v[240:243]\n"
  "v_mfma_f32_16x16x32_bf16 v[244:247], v[36:39], v[220:223], v[244:247]\n"
  "s_waitcnt lgkmcnt(0)\n"
  "v_mfma_f32_16x16x32_bf16 v[252:255], v[40:43], v[220:223], v[252:255]\n"
  "s_barrier\n"
  "v_mfma_f32_16x16x32_bf16 v[248:251], v[40:43], v[216:219], v[248:251]\n"
  "v_max3_f32 v16, v224, v225, v226\n"
  "v_max3_f32 v18, v228, v229, v230\n"
  "v_max3_f32 v16, v16, v227, v232\n"
  "v_max3_f32 v18, v18, v231, v236\n"
  "v_max3_f32 v16, v16, v233, v234\n"
  "v_max3_f32 v18, v18, v237, v238\n"
  "v_max3_f32 v16, v16, v235, v240\n"
  "v_max3_f32 v18, v18, v239, v244\n"
  "v_max3_f32 v16, v16, v241, v242\n"
  "v_max3_f32 v18, v18, v245, v246\n"
  "v_max3_f32 v16, v16, v243, v248\n"
  "v_max3_f32 v18, v18, v247, v252\n"
  "v_max3_f32 v16, v16, v249, v250\n"
  "v_max3_f32 v18, v18, v253, v254\n"
  "v_max_f32 v16, v16, v251\n"
  "v_max_f32 v18, v18, v255\n"
  "s_nop 1\n"
  "v_permlane32_swap_b32 v16, v18\n"
  "v_max_f32 v16, v16, v18\n"
  "v_mov_b32 v17, v16\n"
  "s_nop 1\n"
  "v_permlane16_swap_b32 v16, v17\n"
  "v_max_f32 v16, v16, v17\n"
  "v_mov_b32 v18, v16\n"
  "s_nop 1\n"
  "v_permlane32_swap_b32 v16, v18\n"
  "v_mul_f32 v16, s54, v16\n"
  "v_mul_f32 v18, s54, v18\n"
  "v_pk_fma_f32 v[224:225], v[224:225], s[54:55], v[16:17] op_sel_hi:[1,1,0] neg_lo:[0,0,1] neg_hi:[0,0,1]\n"
  "v_pk_fma_f32 v[228:229], v[228:229], s[54:55], v[18:19] op_sel_hi:[1,1,0] neg_lo:[0,0,1] neg_hi:[0,0,1]\n"
  "v_exp_f32 v224, v224\n"
  "v_exp_f32 v225, v225\n"
  "v_pk_fma_f32 v[226:227], v[226:227], s[54:55], v[16:17] op_sel_hi:[1,1,0] neg_lo:[0,0,1] neg_hi:[0,0,1]\n"
  "v_exp_f32 v228, v228\n"
  "v_exp_f32 v229, v229\n"
  "v_pk_fma_f32 v[230:231], v[230:231], s[54:55], v[18:19] op_sel_hi:[1,1,0] neg_lo:[0,0,1] neg_hi:[0,0,1]\n"
  "v_exp_f32 v226, v226\n"
  "v_exp_f32 v227, v227\n"
  "v_pk_fma_f32 v[232:233], v[232:233], s[54:55], v[16:17] op_sel_hi:[1,1,0] neg_lo:[0,0,1] neg_hi:[0,0,1]\n"
  "v_exp_f32 v230, v230\n"
  "v_exp_f32 v231, v231\n"
  "v_pk_add_f32 v[20:21], v[224:225], v[226:227]\n"
  "v_pk_fma_f32 v[236:237], v[236:237], s[54:55], v[18:19] op_sel_hi:[1,1,0] neg_lo:[0,0,1] neg_hi:[0,0,1]\n"
  "v_exp_f32 v232, v232\n"
  "v_exp_f32 v233, v233\n"
  "v_pk_add_f32 v[22:23], v[228:229], v[230:231]\n"
  "v_pk_fma_f32 v[234:235], v[234:235], s[54:55], v[16:17] op_sel_hi:[1,1,0] neg_lo:[0,0,1] neg_hi:[0,0,1]\n"
  "v_exp_f32 v236, v236\n"
  "v_exp_f32 v237, v237\n"
  "v_pk_add_f32 v[20:21], v[20:21], v[232:233]\n"
  "v_pk_fma_f32 v[238:239], v[238:239], s[54:55], v[18:19] op_sel_hi:[1,1,0] neg_lo:[0,0,1] neg_hi:[0,0,1]\n"
  "v_exp_f32 v234, v234\n"
  "v_exp_f32 v235, v235\n"
  "v_pk_add_f32 v[22:23], v[22:23], v[236:237]\n"
  "v_pk_fma_f32 v[240:241], v[240:241], s[54:55], v[16:17] op_sel_hi:[1,1,0] neg_lo:[0,0,1] neg_hi:[0,0,1]\n"
  "v_exp_f32 v238, v238\n"
  "v_exp_f32 v239, v239\n"
  "v_pk_add_f32 v[20:21], v[20:21], v[234:235]\n"
  "v_pk_fma_f32 v[244:245], v[244:245], s[54:55], v[18:19] op_sel_hi:[1,1,0] neg_lo:[0,0,1] neg_hi:[0,0,1]\n"
  "v_exp_f32 v240, v240\n"
  "v_exp_f32 v241, v241\n"
  "v_pk_add_f32 v[22:23], v[22:23], v[238:239]\n"
  "v_pk_fma_f32 v[242:243], v[242:243], s[54:55], v[16:17] op_sel_hi:[1,1,0] neg_lo:[0,0,1] neg_hi:[0,0,1]\n"
  "v_exp_f32 v244, v244\n"
  "v_exp_f32 v245, v245\n"
  "v_pk_add_f32 v[20:21], v[20:21], v[240:241]\n"
  "v_pk_fma_f32 v[246:247], v[246:247], s[54:55], v[18:19] op_sel_hi:[1,1,0] neg_lo:[0,0,1] neg_hi:[0,0,1]\n"
  "v_exp_f32 v242, v242\n"
  "v_exp_f32 v243, v243\n"
  "v_pk_add_f32 v[22:23], v[22:23], v[244:245]\n"
  "v_pk_fma_f32 v[248:249], v[248:249], s[54:55], v[16:17] op_sel_hi:[1,1,0] neg_lo:[0,0,1] neg_hi:[0,0,1]\n"
  "v_exp_f32 v246, v246\n"
  "v_exp_f32 v247, v247\n"
  "v_pk_add_f32 v[20:21], v[20:21], v[242:243]\n"
  "v_pk_fma_f32 v[252:253], v[252:253], s[54:55], v[18:19] op_sel_hi:[1,1,0] neg_lo:[0,0,1] neg_hi:[0,0,1]\n"
  "v_exp_f32 v248, v248\n"
  "v_exp_f32 v249, v249\n"
  "v_pk_add_f32 v[22:23], v[22:23], v[246:247]\n"
  "v_pk_fma_f32 v[250:251], v[250:251], s[54:55], v[16:17] op_sel_hi:[1,1,0] neg_lo:[0,0,1] neg_hi:[0,0,1]\n"
  "v_exp_f32 v252, v252\n"
  "v_exp_f32 v253, v253\n"
  "v_pk_add_f32 v[20:21], v[20:21], v[248:249]\n"
  "v_pk_fma_f32 v[254:255], v[254:255], s[54:55], v[18:19] op_sel_hi:[1,1,0] neg_lo:[0,0,1] neg_hi:[0,0,1]\n"
  "v_exp_f32 v250, v250\n"
  "v_exp_f32 v251, v251\n"
  "v_pk_add_f32 v[22:23], v[22:23], v[252:253]\n"
  "v_exp_f32 v254, v254\n"
  "v_exp_f32 v255, v255\n"
  "v_pk_add_f32 v[20:21], v[20:21], v[250:251]\n"
  "v_pk_add_f32 v[22:23], v[22:23], v[254:255]\n"
  "v_add_f32 v20, v20, v21\n"
  "v_add_f32 v22, v22, v23\n"
  "s_nop 1\n"
  "v_permlane32_swap_b32 v20, v22\n"
  "v_add_f32 v20, v20, v22\n"
  "v_mov_b32 v17, v20\n"
  "s_nop 1\n"
  "v_permlane16_swap_b32 v20, v17\n"
  "v_add_f32 v20, v20, v17\n"
  "v_mov_b32 v22, v20\n"
  "s_nop 1\n"
  "v_permlane32_swap_b32 v20, v22\n"
  "v_rcp_f32 v16, v20\n"
  "v_rcp_f32 v18, v22\n"
  "v_pk_mul_f32 v[224:225], v[224:225], v[16:17] op_sel_hi:[1,0]\n"
  "v_pk_mul_f32 v[226:227], v[226:227], v[16:17] op_sel_hi:[1,0]\n"
  "v_pk_mul_f32 v[232:233], v[232:233], v[16:17] op_sel_hi:[1,0]\n"
  "v_pk_mul_f32 v[234:235], v[234:235], v[16:17] op_sel_hi:[1,0]\n"
  "v_cvt_pk_bf16_f32 v192, v224, v225\n"
  "v_cvt_pk_bf16_f32 v193, v226, v227\n"
  "v_cvt_pk_bf16_f32 v194, v232, v233\n"
  "v_cvt_pk_bf16_f32 v195, v234, v235\n"
  "v_pk_mul_f32 v[228:229], v[228:229], v[18:19] op_sel_hi:[1,0]\n"
  "v_pk_mul_f32 v[230:231], v[230:231], v[18:19] op_sel_hi:[1,0]\n"
  "s_barrier\n"
  "v_add_u32 v6, 4294901760, v6\n"
  "v_add_u32 v7, 4294901760, v7\n"
  "v_add_u32 v8, 4294901760, v8\n"
  "v_add_u32 v9, 4294901760, v9\n"
  "ds_read_b64_tr_b16 v[44:45], v6 offset:32768\n"
  "ds_read_b64_tr_b16 v[46:47], v6 offset:34816\n"
  "ds_read_b64_tr_b16 v[28:29], v7 offset:32768\n"
  "ds_read_b64_tr_b16 v[30:31], v7 offset:34816\n"
  "ds_read_b64_tr_b16 v[32:33], v8 offset:32768\n"
  "ds_read_b64_tr_b16 v[34:35], v8 offset:34816\n"
  "ds_read_b64_tr_b16 v[36:37], v9 offset:32768\n"
  "ds_read_b64_tr_b16 v[38:39], v9 offset:34816\n"
  "v_pk_mul_f32 v[236:237], v[236:237], v[18:19] op_sel_hi:[1,0]\n"
  "v_pk_mul_f32 v[238:239], v[238:239], v[18:19] op_sel_hi:[1,0]\n"
  "v_cvt_pk_bf16_f32 v196, v228, v229\n"
  "v_cvt_pk_bf16_f32 v197, v230, v231\n"
  "v_cvt_pk_bf16_f32 v198, v236, v237\n"
  "v_cvt_pk_bf16_f32 v199, v238, v239\n"
  "v_pk_mul_f32 v[240:241], v[240:241], v[16:17] op_sel_hi:[1,0]\n"
  "v_pk_mul_f32 v[242:243], v[242:243], v[16:17] op_sel_hi:[1,0]\n"
  "v_pk_mul_f32 v[248:249], v[248:249], v[16:17] op_sel_hi:[1,0]\n"
  "v_pk_mul_f32 v[250:251], v[250:251], v[16:17] op_sel_hi:[1,0]\n"
  "v_cvt_pk_bf16_f32 v200, v240, v241\n"
  "v_cvt_pk_bf16_f32 v201, v242, v243\n"
  "v_cvt_pk_bf16_f32 v202, v248, v249\n"
  "v_cvt_pk_bf16_f32 v203, v250, v251\n"
  "v_pk_mul_f32 v[244:245], v[244:245], v[18:19] op_sel_hi:[1,0]\n"
  "v_pk_mul_f32 v[246:247], v[246:247], v[18:19] op_sel_hi:[1,0]\n"
  "v_pk_mul_f32 v[252:253], v[252:253], v[18:19] op_sel_hi:[1,0]\n"
  "v_pk_mul_f32 v[254:255], v[254:255], v[18:19] op_sel_hi:[1,0]\n"
  "v_cvt_pk_bf16_f32 v204, v244, v245\n"
  "v_cvt_pk_bf16_f32 v205, v246, v247\n"
  "v_cvt_pk_bf16_f32 v206, v252, v253\n"
  "v_cvt_pk_bf16_f32 v207, v254, v255\n"
  "s_waitcnt lgkmcnt(6)\n"
  "v_mfma_f32_16x16x32_bf16 v[128:131], v[44:47], v[192:195], 0\n"
  "ds_read_b64_tr_b16 v[40:41], v6 offset:40960\n"
  "v_mfma_f32_16x16x32_bf16 v[132:135], v[44:47], v[196:199], 0\n"
  "ds_read_b64_tr_b16 v[42:43], v6 offset:43008\n"
  "s_waitcnt lgkmcnt(6)\n"
  "v_mfma_f32_16x16x32_bf16 v[140:143], v[28:31], v[196:199], 0\n"
  "ds_read_b64_tr_b16 v[44:45], v7 offset:40960\n"
  "v_mfma_f32_16x16x32_bf16 v[136:139], v[28:31], v[192:195], 0\n"
  "ds_read_b64_tr_b16 v[46:47], v7 offset:43008\n"
  "s_waitcnt lgkmcnt(6)\n"
  "v_mfma_f32_16x16x32_bf16 v[144:147], v[32:35], v[192:195], 0\n"
  "ds_read_b64_tr_b16 v[28:29], v8 offset:40960\n"
  "v_mfma_f32_16x16x32_bf16 v[148:151], v[32:35], v[196:199], 0\n"
  "ds_read_b64_tr_b16 v[30:31], v8 offset:43008\n"
  "s_waitcnt lgkmcnt(6)\n"
  "v_mfma_f32_16x16x32_bf16 v[156:159], v[36:39], v[196:199], 0\n"
  "ds_read_b64_tr_b16 v[32:33], v9 offset:40960\n"
  "v_mfma_f32_16x16x32_bf16 v[152:155], v[36:39], v[192:195], 0\n"
  "ds_read_b64_tr_b16 v[34:35], v9 offset:43008\n"
  "s_waitcnt lgkmcnt(6)\n"
  "v_mfma_f32_16x16x32_bf16 v[160:163], v[40:43], v[192:195], 0\n"
  "ds_read_b64_tr_b16 v[36:37], v6 offset:36864\n"
  "v_mfma_f32_16x16x32_bf16 v[164:167], v[40:43], v[196:199], 0\n"
  "ds_read_b64_tr_b16 v[38:39], v6 offset:38912\n"
  "s_waitcnt lgkmcnt(6)\n"
  "v_mfma_f32_16x16x32_bf16 v[172:175], v[44:47], v[196:199], 0\n"
  "ds_read_b64_tr_b16 v[40:41], v7 offset:36864\n"
  "v_mfma_f32_16x16x32_bf16 v[168:171], v[44:47], v[192:195], 0\n"
  "ds_read_b64_tr_b16 v[42:43], v7 offset:38912\n"
  "s_waitcnt lgkmcnt(6)\n"
  "v_mfma_f32_16x16x32_bf16 v[176:179], v[28:31], v[192:195], 0\n"
  "ds_read_b64_tr_b16 v[44:45], v8 offset:36864\n"
  "v_mfma_f32_16x16x32_bf16 v[180:183], v[28:31], v[196:199], 0\n"
  "ds_read_b64_tr_b16 v[46:47], v8 offset:38912\n"
  "s_waitcnt lgkmcnt(6)\n"
  "v_mfma_f32_16x16x32_bf16 v[188:191], v[32:35], v[196:199], 0\n"
  "ds_read_b64_tr_b16 v[28:29], v9 offset:36864\n"
  "v_mfma_f32_16x16x32_bf16 v[184:187], v[32:35], v[192:195], 0\n"
  "ds_read_b64_tr_b16 v[30:31], v9 offset:38912\n"
  "s_waitcnt lgkmcnt(6)\n"
  "v_mfma_f32_16x16x32_bf16 v[128:131], v[36:39], v[200:203], v[128:131]\n"
  "ds_read_b64_tr_b16 v[32:33], v6 offset:45056\n"
  "v_mfma_f32_16x16x32_bf16 v[132:135], v[36:39], v[204:207], v[132:135]\n"
  "ds_read_b64_tr_b16 v[34:35], v6 offset:47104\n"
  "s_waitcnt lgkmcnt(6)\n"
  "v_mfma_f32_16x16x32_bf16 v[140:143], v[40:43], v[204:207], v[140:143]\n"
  "ds_read_b64_tr_b16 v[36:37], v7 offset:45056\n"
  "v_mfma_f32_16x16x32_bf16 v[136:139], v[40:43], v[200:203], v[136:139]\n"
  "ds_read_b64_tr_b16 v[38:39], v7 offset:47104\n"
  "s_waitcnt lgkmcnt(6)\n"
  "v_mfma_f32_16x16x32_bf16 v[144:147], v[44:47], v[200:203], v[144:147]\n"
  "ds_read_b64_tr_b16 v[40:41], v8 offset:45056\n"
  "v_mfma_f32_16x16x32_bf16 v[148:151], v[44:47], v[204:207], v[148:151]\n"
  "ds_read_b64_tr_b16 v[42:43], v8 offset:47104\n"
  "s_waitcnt lgkmcnt(6)\n"
  "v_mfma_f32_16x16x32_bf16 v[156:159], v[28:31], v[204:207], v[156:159]\n"
  "ds_read_b64_tr_b16 v[44:45], v9 offset:45056\n"
  "v_mfma_f32_16x16x32_bf16 v[152:155], v[28:31], v[200:203], v[152:155]\n"
  "ds_read_b64_tr_b16 v[46:47], v9 offset:47104\n"
  "s_waitcnt lgkmcnt(6)\n"
  "v_mfma_f32_16x16x32_bf16 v[160:163], v[32:35], v[200:203], v[160:163]\n"
  "s_barrier\n"
  "v_add_u32 v6, 65536, v6\n"
  "v_add_u32 v7, 65536, v7\n"
  "v_add_u32 v8, 65536, v8\n"
  "v_add_u32 v9, 65536, v9\n"
  "v_subrev_u32 v6, s38, v6\n"
  "v_subrev_u32 v7, s38, v7\n"
  "v_subrev_u32 v8, s38, v8\n"
  "v_subrev_u32 v9, s38, v9\n"
  "ds_read_b64_tr_b16 v[28:29], v6 offset:0\n"
  "v_mfma_f32_16x16x32_bf16 v[164:167], v[32:35], v[204:207], v[164:167]\n"
  "ds_read_b64_tr_b16 v[30:31], v6 offset:2048\n"
  "s_waitcnt lgkmcnt(6)\n"
  "v_mfma_f32_16x16x32_bf16 v[172:175], v[36:39], v[204:207], v[172:175]\n"
  "ds_read_b64_tr_b16 v[32:33], v7 offset:0\n"
  "v_mfma_f32_16x16x32_bf16 v[168:171], v[36:39], v[200:203], v[168:171]\n"
  "ds_read_b64_tr_b16 v[34:35], v7 offset:2048\n"
  "s_waitcnt lgkmcnt(6)\n"
  "v_mfma_f32_16x16x32_bf16 v[176:179], v[40:43], v[200:203], v[176:179]\n"
  "ds_read_b64_tr_b16 v[36:37], v8 offset:0\n"
  "v_mfma_f32_16x16x32_bf16 v[180:183], v[40:43], v[204:207], v[180:183]\n"
  "ds_read_b64_tr_b16 v[38:39], v8 offset:2048\n"
  "s_waitcnt lgkmcnt(6)\n"
  "v_mfma_f32_16x16x32_bf16 v[188:191], v[44:47], v[204:207], v[188:191]\n"
  "ds_read_b64_tr_b16 v[40:41], v9 offset:0\n"
  "v_mfma_f32_16x16x32_bf16 v[184:187], v[44:47], v[200:203], v[184:187]\n"
  "ds_read_b64_tr_b16 v[42:43], v9 offset:2048\n"
  "v_cvt_pk_bf16_f32 v224, v128, v129\n"
  "v_cvt_pk_bf16_f32 v225, v130, v131\n"
  "v_cvt_pk_bf16_f32 v226, v136, v137\n"
  "v_cvt_pk_bf16_f32 v227, v138, v139\n"
  "v_cvt_pk_bf16_f32 v228, v132, v133\n"
  "v_cvt_pk_bf16_f32 v229, v134, v135\n"
  "v_cvt_pk_bf16_f32 v230, v140, v141\n"
  "v_cvt_pk_bf16_f32 v231, v142, v143\n"
  "v_cvt_pk_bf16_f32 v232, v144, v145\n"
  "v_cvt_pk_bf16_f32 v233, v146, v147\n"
  "v_cvt_pk_bf16_f32 v234, v152, v153\n"
  "v_cvt_pk_bf16_f32 v235, v154, v155\n"
  "v_cvt_pk_bf16_f32 v236, v148, v149\n"
  "v_cvt_pk_bf16_f32 v237, v150, v151\n"
  "v_cvt_pk_bf16_f32 v238, v156, v157\n"
  "v_cvt_pk_bf16_f32 v239, v158, v159\n"
  "v_cvt_pk_bf16_f32 v240, v160, v161\n"
  "v_cvt_pk_bf16_f32 v241, v162, v163\n"
  "v_cvt_pk_bf16_f32 v242, v168, v169\n"
  "v_cvt_pk_bf16_f32 v243, v170, v171\n"
  "v_cvt_pk_bf16_f32 v244, v164, v165\n"
  "v_cvt_pk_bf16_f32 v245, v166, v167\n"
  "v_cvt_pk_bf16_f32 v246, v172, v173\n"
  "v_cvt_pk_bf16_f32 v247, v174, v175\n"
  "v_cvt_pk_bf16_f32 v248, v176, v177\n"
  "v_cvt_pk_bf16_f32 v249, v178, v179\n"
  "v_cvt_pk_bf16_f32 v250, v184, v185\n"
  "v_cvt_pk_bf16_f32 v251, v186, v187\n"
  "v_cvt_pk_bf16_f32 v252, v180, v181\n"
  "v_cvt_pk_bf16_f32 v253, v182, v183\n"
  "v_cvt_pk_bf16_f32 v254, v188, v189\n"
  "v_cvt_pk_bf16_f32 v255, v190, v191\n"
  "s_add_u32 s39, s67, 32768\n"
  "buffer_store_dwordx4 v[224:227], v1, s[24:27], s39 offen sc1\n"
  "s_waitcnt lgkmcnt(6)\n"
  "v_mfma_f32_16x16x32_bf16 v[128:131], v[28:31], v[64:67], 0\n"
  "ds_read_b64_tr_b16 v[44:45], v6 offset:8192\n"
  "v_mfma_f32_16x16x32_bf16 v[132:135], v[28:31], v[68:71], 0\n"
  "ds_read_b64_tr_b16 v[46:47], v6 offset:10240\n"
  "buffer_store_dwordx4 v[228:231], v1, s[24:27], s39 offen offset:1024 sc1\n"
  "s_waitcnt lgkmcnt(6)\n"
  "v_mfma_f32_16x16x32_bf16 v[140:143], v[32:35], v[68:71], 0\n"
  "ds_read_b64_tr_b16 v[28:29], v7 offset:8192\n"
  "v_mfma_f32_16x16x32_bf16 v[136:139], v[32:35], v[64:67], 0\n"
  "ds_read_b64_tr_b16 v[30:31], v7 offset:10240\n"
  "buffer_store_dwordx4 v[232:235], v1, s[24:27], s39 offen offset:2048 sc1\n"
  "s_waitcnt lgkmcnt(6)\n"
  "v_mfma_f32_16x16x32_bf16 v[144:147], v[36:39], v[64:67], 0\n"
  "ds_read_b64_tr_b16 v[32:33], v8 offset:8192\n"
  "v_mfma_f32_16x16x32_bf16 v[148:151], v[36:39], v[68:71], 0\n"
  "ds_read_b64_tr_b16 v[34:35], v8 offset:10240\n"
  "buffer_store_dwordx4 v[236:239], v1, s[24:27], s39 offen offset:3072 sc1\n"
  "s_waitcnt lgkmcnt(6)\n"
  "v_mfma_f32_16x16x32_bf16 v[156:159], v[40:43], v[68:71], 0\n"
  "ds_read_b64_tr_b16 v[36:37], v9 offset:8192\n"
  "v_mfma_f32_16x16x32_bf16 v[152:155], v[40:43], v[64:67], 0\n"
  "ds_read_b64_tr_b16 v[38:39], v9 offset:10240\n"
  "s_waitcnt lgkmcnt(6)\n"
  "v_mfma_f32_16x16x32_bf16 v[160:163], v[44:47], v[64:67], 0\n"
  "ds_read_b64_tr_b16 v[40:41], v6 offset:4096\n"
  "v_mfma_f32_16x16x32_bf16 v[164:167], v[44:47], v[68:71], 0\n"
  "ds_read_b64_tr_b16 v[42:43], v6 offset:6144\n"
  "s_add_u32 s39, s67, 36864\n"
  "buffer_store_dwordx4 v[240:243], v1, s[24:27], s39 offen sc1\n"
  "s_waitcnt lgkmcnt(6)\n"
  "v_mfma_f32_16x16x32_bf16 v[172:175], v[28:31], v[68:71], 0\n"
  "ds_read_b64_tr_b16 v[44:45], v7 offset:4096\n"
  "v_mfma_f32_16x16x32_bf16 v[168:171], v[28:31], v[64:67], 0\n"
  "ds_read_b64_tr_b16 v[46:47], v7 offset:6144\n"
  "buffer_store_dwordx4 v[244:247], v1, s[24:27], s39 offen offset:1024 sc1\n"
  "s_waitcnt lgkmcnt(6)\n"
  "v_mfma_f32_16x16x32_bf16 v[176:179], v[32:35], v[64:67], 0\n"
  "ds_read_b64_tr_b16 v[28:29], v8 offset:4096\n"
  "v_mfma_f32_16x16x32_bf16 v[180:183], v[32:35], v[68:71], 0\n"
  "ds_read_b64_tr_b16 v[30:31], v8 offset:6144\n"
  "buffer_store_dwordx4 v[248:251], v1, s[24:27], s39 offen offset:2048 sc1\n"
  "s_waitcnt lgkmcnt(6)\n"
  "v_mfma_f32_16x16x32_bf16 v[188:191], v[36:39], v[68:71], 0\n"
  "ds_read_b64_tr_b16 v[32:33], v9 offset:4096\n"
  "v_mfma_f32_16x16x32_bf16 v[184:187], v[36:39], v[64:67], 0\n"
  "ds_read_b64_tr_b16 v[34:35], v9 offset:6144\n"
  "buffer_store_dwordx4 v[252:255], v1, s[24:27], s39 offen offset:3072 sc1\n"
  "s_waitcnt lgkmcnt(6)\n"
  "v_mfma_f32_16x16x32_bf16 v[128:131], v[40:43], v[72:75], v[128:131]\n"
  "ds_read_b64_tr_b16 v[36:37], v6 offset:12288\n"
  "v_mfma_f32_16x16x32_bf16 v[132:135], v[40:43], v[76:79], v[132:135]\n"
  "ds_read_b64_tr_b16 v[38:39], v6 offset:14336\n"
  "s_waitcnt lgkmcnt(6)\n"
  "v_mfma_f32_16x16x32_bf16 v[140:143], v[44:47], v[76:79], v[140:143]\n"
  "ds_read_b64_tr_b16 v[40:41], v7 offset:12288\n"
  "v_mfma_f32_16x16x32_bf16 v[136:139], v[44:47], v[72:75], v[136:139]\n"
  "ds_read_b64_tr_b16 v[42:43], v7 offset:14336\n"
  "s_waitcnt lgkmcnt(6)\n"
  "v_mfma_f32_16x16x32_bf16 v[144:147], v[28:31], v[72:75], v[144:147]\n"
  "ds_read_b64_tr_b16 v[44:45], v8 offset:12288\n"
  "v_mfma_f32_16x16x32_bf16 v[148:151], v[28:31], v[76:79], v[148:151]\n"
  "ds_read_b64_tr_b16 v[46:47], v8 offset:14336\n"
  "s_waitcnt lgkmcnt(6)\n"
  "v_mfma_f32_16x16x32_bf16 v[156:159], v[32:35], v[76:79], v[156:159]\n"
  "ds_read_b64_tr_b16 v[28:29], v9 offset:12288\n"
  "v_mfma_f32_16x16x32_bf16 v[152:155], v[32:35], v[72:75], v[152:155]\n"
  "ds_read_b64_tr_b16 v[30:31], v9 offset:14336\n"
  "s_waitcnt lgkmcnt(6)\n"
  "v_mfma_f32_16x16x32_bf16 v[160:163], v[36:39], v[72:75], v[160:163]\n"
  "s_barrier\n"
  "ds_read_b64_tr_b16 v[32:33], v6 offset:16384\n"
  "v_mfma_f32_16x16x32_bf16 v[164:167], v[36:39], v[76:79], v[164:167]\n"
  "ds_read_b64_tr_b16 v[34:35], v6 offset:18432\n"
  "s_waitcnt lgkmcnt(6)\n"
  "v_mfma_f32_16x16x32_bf16 v[172:175], v[40:43], v[76:79], v[172:175]\n"
  "ds_read_b64_tr_b16 v[36:37], v7 offset:16384\n"
  "v_mfma_f32_16x16x32_bf16 v[168:171], v[40:43], v[72:75], v[168:171]\n"
  "ds_read_b64_tr_b16 v[38:39], v7 offset:18432\n"
  "s_waitcnt lgkmcnt(6)\n"
  "v_mfma_f32_16x16x32_bf16 v[176:179], v[44:47], v[72:75], v[176:179]\n"
  "ds_read_b64_tr_b16 v[40:41], v8 offset:16384\n"
  "v_mfma_f32_16x16x32_bf16 v[180:183], v[44:47], v[76:79], v[180:183]\n"
  "ds_read_b64_tr_b16 v[42:43], v8 offset:18432\n"
  "s_waitcnt lgkmcnt(6)\n"
  "v_mfma_f32_16x16x32_bf16 v[188:191], v[28:31], v[76:79], v[188:191]\n"
  "ds_read_b64_tr_b16 v[44:45], v9 offset:16384\n"
  "v_mfma_f32_16x16x32_bf16 v[184:187], v[28:31], v[72:75], v[184:187]\n"
  "ds_read_b64_tr_b16 v[46:47], v9 offset:18432\n"
  "s_waitcnt lgkmcnt(6)\n"
  "v_mfma_f32_16x16x32_bf16 v[128:131], v[32:35], v[80:83], v[128:131]\n"
  "ds_read_b64_tr_b16 v[28:29], v6 offset:24576\n"
  "v_mfma_f32_16x16x32_bf16 v[132:135], v[32:35], v[84:87], v[132:135]\n"
  "ds_read_b64_tr_b16 v[30:31], v6 offset:26624\n"
  "s_waitcnt lgkmcnt(6)\n"
  "v_mfma_f32_16x16x32_bf16 v[140:143], v[36:39], v[84:87], v[140:143]\n"
  "ds_read_b64_tr_b16 v[32:33], v7 offset:24576\n"
  "v_mfma_f32_16x16x32_bf16 v[136:139], v[36:39], v[80:83], v[136:139]\n"
  "ds_read_b64_tr_b16 v[34:35], v7 offset:26624\n"
  "s_waitcnt lgkmcnt(6)\n"
  "v_mfma_f32_16x16x32_bf16 v[144:147], v[40:43], v[80:83], v[144:147]\n"
  "ds_read_b64_tr_b16 v[36:37], v8 offset:24576\n"
  "v_mfma_f32_16x16x32_bf16 v[148:151], v[40:43], v[84:87], v[148:151]\n"
  "ds_read_b64_tr_b16 v[38:39], v8 offset:26624\n"
  "s_waitcnt lgkmcnt(6)\n"
  "v_mfma_f32_16x16x32_bf16 v[156:159], v[44:47], v[84:87], v[156:159]\n"
  "ds_read_b64_tr_b16 v[40:41], v9 offset:24576\n"
  "v_mfma_f32_16x16x32_bf16 v[152:155], v[44:47], v[80:83], v[152:155]\n"
  "ds_read_b64_tr_b16 v[42:43], v9 offset:26624\n"
  "s_waitcnt lgkmcnt(6)\n"
  "v_mfma_f32_16x16x32_bf16 v[160:163], v[28:31], v[80:83], v[160:163]\n"
  "ds_read_b64_tr_b16 v[44:45], v6 offset:20480\n"
  "v_mfma_f32_16x16x32_bf16 v[164:167], v[28:31], v[84:87], v[164:167]\n"
  "ds_read_b64_tr_b16 v[46:47], v6 offset:22528\n"
  "s_waitcnt lgkmcnt(6)\n"
  "v_mfma_f32_16x16x32_bf16 v[172:175], v[32:35], v[84:87], v[172:175]\n"
  "ds_read_b64_tr_b16 v[28:29], v7 offset:20480\n"
  "v_mfma_f32_16x16x32_bf16 v[168:171], v[32:35], v[80:83], v[168:171]\n"
  "ds_read_b64_tr_b16 v[30:31], v7 offset:22528\n"
  "s_waitcnt lgkmcnt(6)\n"
  "v_mfma_f32_16x16x32_bf16 v[176:179], v[36:39], v[80:83], v[176:179]\n"
  "ds_read_b64_tr_b16 v[32:33], v8 offset:20480\n"
  "v_mfma_f32_16x16x32_bf16 v[180:183], v[36:39], v[84:87], v[180:183]\n"
  "ds_read_b64_tr_b16 v[34:35], v8 offset:22528\n"
  "s_waitcnt lgkmcnt(6)\n"
  "v_mfma_f32_16x16x32_bf16 v[188:191], v[40:43], v[84:87], v[188:191]\n"
  "ds_read_b64_tr_b16 v[36:37], v9 offset:20480\n"
  "v_mfma_f32_16x16x32_bf16 v[184:187], v[40:43], v[80:83], v[184:187]\n"
  "ds_read_b64_tr_b16 v[38:39], v9 offset:22528\n"
  "s_waitcnt lgkmcnt(6)\n"
  "v_mfma_f32_16x16x32_bf16 v[128:131], v[44:47], v[88:91], v[128:131]\n"
  "ds_read_b64_tr_b16 v[40:41], v6 offset:28672\n"
  "v_mfma_f32_16x16x32_bf16 v[132:135], v[44:47], v[92:95], v[132:135]\n"
  "ds_read_b64_tr_b16 v[42:43], v6 offset:30720\n"
  "s_waitcnt lgkmcnt(6)\n"
  "v_mfma_f32_16x16x32_bf16 v[140:143], v[28:31], v[92:95], v[140:143]\n"
  "ds_read_b64_tr_b16 v[44:45], v7 offset:28672\n"
  "v_mfma_f32_16x16x32_bf16 v[136:139], v[28:31], v[88:91], v[136:139]\n"
  "ds_read_b64_tr_b16 v[46:47], v7 offset:30720\n"
  "s_waitcnt lgkmcnt(6)\n"
  "v_mfma_f32_16x16x32_bf16 v[144:147], v[32:35], v[88:91], v[144:147]\n"
  "ds_read_b64_tr_b16 v[28:29], v8 offset:28672\n"
  "v_mfma_f32_16x16x32_bf16 v[148:151], v[32:35], v[92:95], v[148:151]\n"
  "ds_read_b64_tr_b16 v[30:31], v8 offset:30720\n"
  "s_waitcnt lgkmcnt(6)\n"
  "v_mfma_f32_16x16x32_bf16 v[156:159], v[36:39], v[92:95], v[156:159]\n"
  "ds_read_b64_tr_b16 v[32:33], v9 offset:28672\n"
  "v_mfma_f32_16x16x32_bf16 v[152:155], v[36:39], v[88:91], v[152:155]\n"
  "ds_read_b64_tr_b16 v[34:35], v9 offset:30720\n"
  "s_waitcnt lgkmcnt(6)\n"
  "v_mfma_f32_16x16x32_bf16 v[160:163], v[40:43], v[88:91], v[160:163]\n"
  "s_barrier\n"
  "v_add_u32 v6, 4294836224, v6\n"
  "v_add_u32 v7, 4294836224, v7\n"
  "v_add_u32 v8, 4294836224, v8\n"
  "v_add_u32 v9, 4294836224, v9\n"
  "ds_read_b64_tr_b16 v[36:37], v6 offset:32768\n"
  "v_mfma_f32_16x16x32_bf16 v[164:167], v[40:43], v[92:95], v[164:167]\n"
  "ds_read_b64_tr_b16 v[38:39], v6 offset:34816\n"
  "s_waitcnt lgkmcnt(6)\n"
  "v_mfma_f32_16x16x32_bf16 v[172:175], v[44:47], v[92:95], v[172:175]\n"
  "ds_read_b64_tr_b16 v[40:41], v7 offset:32768\n"
  "v_mfma_f32_16x16x32_bf16 v[168:171], v[44:47], v[88:91], v[168:171]\n"
  "ds_read_b64_tr_b16 v[42:43], v7 offset:34816\n"
  "s_waitcnt lgkmcnt(6)\n"
  "v_mfma_f32_16x16x32_bf16 v[176:179], v[28:31], v[88:91], v[176:179]\n"
  "ds_read_b64_tr_b16 v[44:45], v8 offset:32768\n"
  "v_mfma_f32_16x16x32_bf16 v[180:183], v[28:31], v[92:95], v[180:183]\n"
  "ds_read_b64_tr_b16 v[46:47], v8 offset:34816\n"
  "s_waitcnt lgkmcnt(6)\n"
  "v_mfma_f32_16x16x32_bf16 v[188:191], v[32:35], v[92:95], v[188:191]\n"
  "ds_read_b64_tr_b16 v[28:29], v9 offset:32768\n"
  "v_mfma_f32_16x16x32_bf16 v[184:187], v[32:35], v[88:91], v[184:187]\n"
  "ds_read_b64_tr_b16 v[30:31], v9 offset:34816\n"
  "s_waitcnt lgkmcnt(6)\n"
  "v_mfma_f32_16x16x32_bf16 v[128:131], v[36:39], v[224:227], v[128:131]\n"
  "ds_read_b64_tr_b16 v[32:33], v6 offset:40960\n"
  "v_mfma_f32_16x16x32_bf16 v[132:135], v[36:39], v[228:231], v[132:135]\n"
  "ds_read_b64_tr_b16 v[34:35], v6 offset:43008\n"
  "v_and_b32 v14, 15, v0\n"
  "v_lshlrev_b32 v14, 11, v14\n"
  "v_bfe_u32 v15, v0, 4, 1\n"
  "v_lshl_add_u32 v14, v15, 5, v14\n"
  "v_lshrrev_b32 v15, 5, v0\n"
  "v_lshl_add_u32 v14, v15, 4, v14\n"
  "s_lshl_b32 s42, s37, 4\n"
  "v_add_u32 v14, s42, v14\n"
  "s_mov_b32 s43, 32768\n"
  "s_waitcnt lgkmcnt(6)\n"
  "v_mfma_f32_16x16x32_bf16 v[140:143], v[40:43], v[228:231], v[140:143]\n"
  "ds_read_b64_tr_b16 v[36:37], v7 offset:40960\n"
  "v_mfma_f32_16x16x32_bf16 v[136:139], v[40:43], v[224:227], v[136:139]\n"
  "ds_read_b64_tr_b16 v[38:39], v7 offset:43008\n"
  "s_waitcnt lgkmcnt(6)\n"
  "v_mfma_f32_16x16x32_bf16 v[144:147], v[44:47], v[224:227], v[144:147]\n"
  "ds_read_b64_tr_b16 v[40:41], v8 offset:40960\n"
  "v_mfma_f32_16x16x32_bf16 v[148:151], v[44:47], v[228:231], v[148:151]\n"
  "ds_read_b64_tr_b16 v[42:43], v8 offset:43008\n"
  "s_waitcnt lgkmcnt(6)\n"
  "v_mfma_f32_16x16x32_bf16 v[156:159], v[28:31], v[228:231], v[156:159]\n"
  "ds_read_b64_tr_b16 v[44:45], v9 offset:40960\n"
  "v_mfma_f32_16x16x32_bf16 v[152:155], v[28:31], v[224:227], v[152:155]\n"
  "ds_read_b64_tr_b16 v[46:47], v9 offset:43008\n"
  "s_waitcnt lgkmcnt(6)\n"
  "v_mfma_f32_16x16x32_bf16 v[160:163], v[32:35], v[224:227], v[160:163]\n"
  "ds_read_b64_tr_b16 v[28:29], v6 offset:36864\n"
  "v_mfma_f32_16x16x32_bf16 v[164:167], v[32:35], v[228:231], v[164:167]\n"
  "ds_read_b64_tr_b16 v[30:31], v6 offset:38912\n"
  "s_waitcnt lgkmcnt(6)\n"
  "v_mfma_f32_16x16x32_bf16 v[172:175], v[36:39], v[228:231], v[172:175]\n"
  "ds_read_b64_tr_b16 v[32:33], v7 offset:36864\n"
  "v_mfma_f32_16x16x32_bf16 v[168:171], v[36:39], v[224:227], v[168:171]\n"
  "ds_read_b64_tr_b16 v[34:35], v7 offset:38912\n"
  "s_waitcnt lgkmcnt(6)\n"
  "v_mfma_f32_16x16x32_bf16 v[176:179], v[40:43], v[224:227], v[176:179]\n"
  "ds_read_b64_tr_b16 v[36:37], v8 offset:36864\n"
  "v_mfma_f32_16x16x32_bf16 v[180:183], v[40:43], v[228:231], v[180:183]\n"
  "ds_read_b64_tr_b16 v[38:39], v8 offset:38912\n"
  "s_waitcnt lgkmcnt(6)\n"
  "v_mfma_f32_16x16x32_bf16 v[188:191], v[44:47], v[228:231], v[188:191]\n"
  "ds_read_b64_tr_b16 v[40:41], v9 offset:36864\n"
  "v_mfma_f32_16x16x32_bf16 v[184:187], v[44:47], v[224:227], v[184:187]\n"
  "ds_read_b64_tr_b16 v[42:43], v9 offset:38912\n"
  "s_waitcnt lgkmcnt(6)\n"
  "v_mfma_f32_16x16x32_bf16 v[128:131], v[28:31], v[232:235], v[128:131]\n"
  "ds_read_b64_tr_b16 v[44:45], v6 offset:45056\n"
  "v_mfma_f32_16x16x32_bf16 v[132:135], v[28:31], v[236:239], v[132:135]\n"
  "ds_read_b64_tr_b16 v[46:47], v6 offset:47104\n"
  "s_waitcnt lgkmcnt(6)\n"
  "v_mfma_f32_16x16x32_bf16 v[140:143], v[32:35], v[236:239], v[140:143]\n"
  "ds_read_b64_tr_b16 v[28:29], v7 offset:45056\n"
  "v_mfma_f32_16x16x32_bf16 v[136:139], v[32:35], v[232:235], v[136:139]\n"
  "ds_read_b64_tr_b16 v[30:31], v7 offset:47104\n"
  "s_waitcnt lgkmcnt(6)\n"
  "v_mfma_f32_16x16x32_bf16 v[144:147], v[36:39], v[232:235], v[144:147]\n"
  "ds_read_b64_tr_b16 v[32:33], v8 offset:45056\n"
  "v_mfma_f32_16x16x32_bf16 v[148:151], v[36:39], v[236:239], v[148:151]\n"
  "ds_read_b64_tr_b16 v[34:35], v8 offset:47104\n"
  "s_waitcnt lgkmcnt(6)\n"
  "v_mfma_f32_16x16x32_bf16 v[156:159], v[40:43], v[236:239], v[156:159]\n"
  "ds_read_b64_tr_b16 v[36:37], v9 offset:45056\n"
  "v_mfma_f32_16x16x32_bf16 v[152:155], v[40:43], v[232:235], v[152:155]\n"
  "ds_read_b64_tr_b16 v[38:39], v9 offset:47104\n"
  "s_waitcnt lgkmcnt(6)\n"
  "v_mfma_f32_16x16x32_bf16 v[160:163], v[44:47], v[232:235], v[160:163]\n"
  "s_barrier\n"
  "ds_read_b64_tr_b16 v[40:41], v6 offset:49152\n"
  "v_mfma_f32_16x16x32_bf16 v[164:167], v[44:47], v[236:239], v[164:167]\n"
  "ds_read_b64_tr_b16 v[42:43], v6 offset:51200\n"
  "s_waitcnt lgkmcnt(6)\n"
  "v_mfma_f32_16x16x32_bf16 v[172:175], v[28:31], v[236:239], v[172:175]\n"
  "ds_read_b64_tr_b16 v[44:45], v7 offset:49152\n"
  "v_mfma_f32_16x16x32_bf16 v[168:171], v[28:31], v[232:235], v[168:171]\n"
  "ds_read_b64_tr_b16 v[46:47], v7 offset:51200\n"
  "s_waitcnt lgkmcnt(6)\n"
  "v_mfma_f32_16x16x32_bf16 v[176:179], v[32:35], v[232:235], v[176:179]\n"
  "ds_read_b64_tr_b16 v[28:29], v8 offset:49152\n"
  "v_mfma_f32_16x16x32_bf16 v[180:183], v[32:35], v[236:239], v[180:183]\n"
  "ds_read_b64_tr_b16 v[30:31], v8 offset:51200\n"
  "s_waitcnt lgkmcnt(6)\n"
  "v_mfma_f32_16x16x32_bf16 v[188:191], v[36:39], v[236:239], v[188:191]\n"
  "ds_read_b64_tr_b16 v[32:33], v9 offset:49152\n"
  "v_mfma_f32_16x16x32_bf16 v[184:187], v[36:39], v[232:235], v[184:187]\n"
  "ds_read_b64_tr_b16 v[34:35], v9 offset:51200\n"
  "s_waitcnt lgkmcnt(6)\n"
  "v_mfma_f32_16x16x32_bf16 v[128:131], v[40:43], v[240:243], v[128:131]\n"
  "ds_read_b64_tr_b16 v[36:37], v6 offset:57344\n"
  "v_mfma_f32_16x16x32_bf16 v[132:135], v[40:43], v[244:247], v[132:135]\n"
  "ds_read_b64_tr_b16 v[38:39], v6 offset:59392\n"
  "s_waitcnt lgkmcnt(6)\n"
  "v_mfma_f32_16x16x32_bf16 v[140:143], v[44:47], v[244:247], v[140:143]\n"
  "ds_read_b64_tr_b16 v[40:41], v7 offset:57344\n"
  "v_mfma_f32_16x16x32_bf16 v[136:139], v[44:47], v[240:243], v[136:139]\n"
  "ds_read_b64_tr_b16 v[42:43], v7 offset:59392\n"
  "s_waitcnt lgkmcnt(6)\n"
  "v_mfma_f32_16x16x32_bf16 v[144:147], v[28:31], v[240:243], v[144:147]\n"
  "ds_read_b64_tr_b16 v[44:45], v8 offset:57344\n"
  "v_mfma_f32_16x16x32_bf16 v[148:151], v[28:31], v[244:247], v[148:151]\n"
  "ds_read_b64_tr_b16 v[46:47], v8 offset:59392\n"
  "s_waitcnt lgkmcnt(6)\n"
  "v_mfma_f32_16x16x32_bf16 v[156:159], v[32:35], v[244:247], v[156:159]\n"
  "ds_read_b64_tr_b16 v[28:29], v9 offset:57344\n"
  "v_mfma_f32_16x16x32_bf16 v[152:155], v[32:35], v[240:243], v[152:155]\n"
  "ds_read_b64_tr_b16 v[30:31], v9 offset:59392\n"
  "s_waitcnt lgkmcnt(6)\n"
  "v_mfma_f32_16x16x32_bf16 v[160:163], v[36:39], v[240:243], v[160:163]\n"
  "ds_read_b64_tr_b16 v[32:33], v6 offset:53248\n"
  "v_mfma_f32_16x16x32_bf16 v[164:167], v[36:39], v[244:247], v[164:167]\n"
  "ds_read_b64_tr_b16 v[34:35], v6 offset:55296\n"
  "s_waitcnt lgkmcnt(6)\n"
  "v_mfma_f32_16x16x32_bf16 v[172:175], v[40:43], v[244:247], v[172:175]\n"
  "ds_read_b64_tr_b16 v[36:37], v7 offset:53248\n"
  "v_mfma_f32_16x16x32_bf16 v[168:171], v[40:43], v[240:243], v[168:171]\n"
  "ds_read_b64_tr_b16 v[38:39], v7 offset:55296\n"
  "s_waitcnt lgkmcnt(6)\n"
  "v_mfma_f32_16x16x32_bf16 v[176:179], v[44:47], v[240:243], v[176:179]\n"
  "ds_read_b64_tr_b16 v[40:41], v8 offset:53248\n"
  "v_mfma_f32_16x16x32_bf16 v[180:183], v[44:47], v[244:247], v[180:183]\n"
  "ds_read_b64_tr_b16 v[42:43], v8 offset:55296\n"
  "s_waitcnt lgkmcnt(6)\n"
  "v_mfma_f32_16x16x32_bf16 v[188:191], v[28:31], v[244:247], v[188:191]\n"
  "ds_read_b64_tr_b16 v[44:45], v9 offset:53248\n"
  "v_mfma_f32_16x16x32_bf16 v[184:187], v[28:31], v[240:243], v[184:187]\n"
  "ds_read_b64_tr_b16 v[46:47], v9 offset:55296\n"
  "s_waitcnt lgkmcnt(6)\n"
  "v_mfma_f32_16x16x32_bf16 v[128:131], v[32:35], v[248:251], v[128:131]\n"
  "ds_read_b64_tr_b16 v[28:29], v6 offset:61440\n"
  "v_mfma_f32_16x16x32_bf16 v[132:135], v[32:35], v[252:255], v[132:135]\n"
  "ds_read_b64_tr_b16 v[30:31], v6 offset:63488\n"
  "s_mov_b64 exec, 1\n"
  "s_waitcnt vmcnt(0)\n"
  "buffer_store_dwordx2 v[26:27], v24, s[24:27], s69 offen offset:8 sc1\n"
  "s_mov_b64 exec, -1\n"
  "s_waitcnt lgkmcnt(6)\n"
  "v_mfma_f32_16x16x32_bf16 v[140:143], v[36:39], v[252:255], v[140:143]\n"
  "ds_read_b64_tr_b16 v[32:33], v7 offset:61440\n"
  "v_mfma_f32_16x16x32_bf16 v[136:139], v[36:39], v[248:251], v[136:139]\n"
  "ds_read_b64_tr_b16 v[34:35], v7 offset:63488\n"
  "s_waitcnt lgkmcnt(6)\n"
  "v_mfma_f32_16x16x32_bf16 v[144:147], v[40:43], v[248:251], v[144:147]\n"
  "ds_read_b64_tr_b16 v[36:37], v8 offset:61440\n"
  "v_mfma_f32_16x16x32_bf16 v[148:151], v[40:43], v[252:255], v[148:151]\n"
  "ds_read_b64_tr_b16 v[38:39], v8 offset:63488\n"
  "s_waitcnt lgkmcnt(6)\n"
  "v_mfma_f32_16x16x32_bf16 v[156:159], v[44:47], v[252:255], v[156:159]\n"
  "ds_read_b64_tr_b16 v[40:41], v9 offset:61440\n"
  "v_mfma_f32_16x16x32_bf16 v[152:155], v[44:47], v[248:251], v[152:155]\n"
  "ds_read_b64_tr_b16 v[42:43], v9 offset:63488\n"
  "s_waitcnt lgkmcnt(6)\n"
  "v_mfma_f32_16x16x32_bf16 v[160:163], v[28:31], v[248:251], v[160:163]\n"
  "s_barrier\n"
  "ds_read_b64_tr_b16 v[44:45], v6 offset:0\n"
  "v_mfma_f32_16x16x32_bf16 v[164:167], v[28:31], v[252:255], v[164:167]\n"
  "ds_read_b64_tr_b16 v[46:47], v6 offset:2048\n"
  "v_add_u32 v12, 65536, v12\n"
  "ds_read_b128 v[48:51], v12 offset:0\n"
  "ds_read_b128 v[52:55], v12 offset:1024\n"
  "s_waitcnt lgkmcnt(8)\n"
  "v_mfma_f32_16x16x32_bf16 v[172:175], v[32:35], v[252:255], v[172:175]\n"
  "ds_read_b64_tr_b16 v[28:29], v7 offset:0\n"
  "v_mfma_f32_16x16x32_bf16 v[168:171], v[32:35], v[248:251], v[168:171]\n"
  "ds_read_b64_tr_b16 v[30:31], v7 offset:2048\n"
  "s_waitcnt lgkmcnt(8)\n"
  "v_mfma_f32_16x16x32_bf16 v[176:179], v[36:39], v[248:251], v[176:179]\n"
  "ds_read_b64_tr_b16 v[32:33], v8 offset:0\n"
  "v_mfma_f32_16x16x32_bf16 v[180:183], v[36:39], v[252:255], v[180:183]\n"
  "ds_read_b64_tr_b16 v[34:35], v8 offset:2048\n"
  "s_waitcnt lgkmcnt(8)\n"
  "v_mfma_f32_16x16x32_bf16 v[188:191], v[40:43], v[252:255], v[188:191]\n"
  "ds_read_b64_tr_b16 v[36:37], v9 offset:0\n"
  "v_mfma_f32_16x16x32_bf16 v[184:187], v[40:43], v[248:251], v[184:187]\n"
  "ds_read_b64_tr_b16 v[38:39], v9 offset:2048\n"
  "s_waitcnt lgkmcnt(7)\n"
  "v_mfma_f32_16x16x32_bf16 v[128:131], v[44:47], v[48:51], v[128:131]\n"
  "ds_read_b64_tr_b16 v[40:41], v6 offset:8192\n"
  "s_waitcnt lgkmcnt(7)\n"
  "v_mfma_f32_16x16x32_bf16 v[132:135], v[44:47], v[52:55], v[132:135]\n"
  "ds_read_b64_tr_b16 v[42:43], v6 offset:10240\n"
  "ds_read_b128 v[56:59], v12 offset:2048\n"
  "ds_read_b128 v[60:63], v12 offset:3072\n"
  "s_waitcnt lgkmcnt(8)\n"
  "v_mfma_f32_16x16x32_bf16 v[140:143], v[28:31], v[52:55], v[140:143]\n"
  "ds_read_b64_tr_b16 v[44:45], v7 offset:8192\n"
  "v_mfma_f32_16x16x32_bf16 v[136:139], v[28:31], v[48:51], v[136:139]\n"
  "ds_read_b64_tr_b16 v[46:47], v7 offset:10240\n"
  "s_waitcnt lgkmcnt(8)\n"
  "v_mfma_f32_16x16x32_bf16 v[144:147], v[32:35], v[48:51], v[144:147]\n"
  "ds_read_b64_tr_b16 v[28:29], v8 offset:8192\n"
  "v_mfma_f32_16x16x32_bf16 v[148:151], v[32:35], v[52:55], v[148:151]\n"
  "ds_read_b64_tr_b16 v[30:31], v8 offset:10240\n"
  "s_waitcnt lgkmcnt(8)\n"
  "v_mfma_f32_16x16x32_bf16 v[156:159], v[36:39], v[52:55], v[156:159]\n"
  "ds_read_b64_tr_b16 v[32:33], v9 offset:8192\n"
  "v_mfma_f32_16x16x32_bf16 v[152:155], v[36:39], v[48:51], v[152:155]\n"
  "ds_read_b64_tr_b16 v[34:35], v9 offset:10240\n"
  "s_waitcnt lgkmcnt(8)\n"
  "v_mfma_f32_16x16x32_bf16 v[160:163], v[40:43], v[48:51], v[160:163]\n"
  "ds_read_b64_tr_b16 v[36:37], v6 offset:4096\n"
  "v_mfma_f32_16x16x32_bf16 v[164:167], v[40:43], v[52:55], v[164:167]\n"
  "ds_read_b64_tr_b16 v[38:39], v6 offset:6144\n"
  "s_waitcnt lgkmcnt(6)\n"
  "v_mfma_f32_16x16x32_bf16 v[172:175], v[44:47], v[52:55], v[172:175]\n"
  "ds_read_b64_tr_b16 v[40:41], v7 offset:4096\n"
  "v_mfma_f32_16x16x32_bf16 v[168:171], v[44:47], v[48:51], v[168:171]\n"
  "ds_read_b64_tr_b16 v[42:43], v7 offset:6144\n"
  "s_waitcnt lgkmcnt(6)\n"
  "v_mfma_f32_16x16x32_bf16 v[176:179], v[28:31], v[48:51], v[176:179]\n"
  "ds_read_b64_tr_b16 v[44:45], v8 offset:4096\n"
  "v_mfma_f32_16x16x32_bf16 v[180:183], v[28:31], v[52:55], v[180:183]\n"
  "ds_read_b64_tr_b16 v[46:47], v8 offset:6144\n"
  "s_waitcnt lgkmcnt(6)\n"
  "v_mfma_f32_16x16x32_bf16 v[188:191], v[32:35], v[52:55], v[188:191]\n"
  "ds_read_b64_tr_b16 v[28:29], v9 offset:4096\n"
  "v_mfma_f32_16x16x32_bf16 v[184:187], v[32:35], v[48:51], v[184:187]\n"
  "ds_read_b64_tr_b16 v[30:31], v9 offset:6144\n"
  "s_waitcnt lgkmcnt(6)\n"
  "v_mfma_f32_16x16x32_bf16 v[128:131], v[36:39], v[56:59], v[128:131]\n"
  "ds_read_b64_tr_b16 v[32:33], v6 offset:12288\n"
  "v_mfma_f32_16x16x32_bf16 v[132:135], v[36:39], v[60:63], v[132:135]\n"
  "ds_read_b64_tr_b16 v[34:35], v6 offset:14336\n"
  "s_waitcnt lgkmcnt(6)\n"
  "v_mfma_f32_16x16x32_bf16 v[140:143], v[40:43], v[60:63], v[140:143]\n"
  "ds_read_b64_tr_b16 v[36:37], v7 offset:12288\n"
  "v_mfma_f32_16x16x32_bf16 v[136:139], v[40:43], v[56:59], v[136:139]\n"
  "ds_read_b64_tr_b16 v[38:39], v7 offset:14336\n"
  "s_waitcnt lgkmcnt(6)\n"
  "v_mfma_f32_16x16x32_bf16 v[144:147], v[44:47], v[56:59], v[144:147]\n"
  "ds_read_b64_tr_b16 v[40:41], v8 offset:12288\n"
  "v_mfma_f32_16x16x32_bf16 v[148:151], v[44:47], v[60:63], v[148:151]\n"
  "ds_read_b64_tr_b16 v[42:43], v8 offset:14336\n"
  "s_waitcnt lgkmcnt(6)\n"
  "v_mfma_f32_16x16x32_bf16 v[156:159], v[28:31], v[60:63], v[156:159]\n"
  "ds_read_b64_tr_b16 v[44:45], v9 offset:12288\n"
  "v_mfma_f32_16x16x32_bf16 v[152:155], v[28:31], v[56:59], v[152:155]\n"
  "ds_read_b64_tr_b16 v[46:47], v9 offset:14336\n"
  "s_waitcnt lgkmcnt(6)\n"
  "v_mfma_f32_16x16x32_bf16 v[160:163], v[32:35], v[56:59], v[160:163]\n"
  "s_barrier\n"
  "ds_read_b64_tr_b16 v[28:29], v6 offset:16384\n"
  "v_mfma_f32_16x16x32_bf16 v[164:167], v[32:35], v[60:63], v[164:167]\n"
  "ds_read_b64_tr_b16 v[30:31], v6 offset:18432\n"
  "ds_read_b128 v[48:51], v12 offset:16384\n"
  "ds_read_b128 v[52:55], v12 offset:17408\n"
  "s_waitcnt lgkmcnt(8)\n"
  "v_mfma_f32_16x16x32_bf16 v[172:175], v[36:39], v[60:63], v[172:175]\n"
  "ds_read_b64_tr_b16 v[32:33], v7 offset:16384\n"
  "v_mfma_f32_16x16x32_bf16 v[168:171], v[36:39], v[56:59], v[168:171]\n"
  "ds_read_b64_tr_b16 v[34:35], v7 offset:18432\n"
  "s_waitcnt lgkmcnt(8)\n"
  "v_mfma_f32_16x16x32_bf16 v[176:179], v[40:43], v[56:59], v[176:179]\n"
  "ds_read_b64_tr_b16 v[36:37], v8 offset:16384\n"
  "v_mfma_f32_16x16x32_bf16 v[180:183], v[40:43], v[60:63], v[180:183]\n"
  "ds_read_b64_tr_b16 v[38:39], v8 offset:18432\n"
  "s_waitcnt lgkmcnt(8)\n"
  "v_mfma_f32_16x16x32_bf16 v[188:191], v[44:47], v[60:63], v[188:191]\n"
  "ds_read_b64_tr_b16 v[40:41], v9 offset:16384\n"
  "v_mfma_f32_16x16x32_bf16 v[184:187], v[44:47], v[56:59], v[184:187]\n"
  "ds_read_b64_tr_b16 v[42:43], v9 offset:18432\n"
  "s_waitcnt lgkmcnt(7)\n"
  "v_mfma_f32_16x16x32_bf16 v[128:131], v[28:31], v[48:51], v[128:131]\n"
  "ds_read_b64_tr_b16 v[44:45], v6 offset:24576\n"
  "s_waitcnt lgkmcnt(7)\n"
  "v_mfma_f32_16x16x32_bf16 v[132:135], v[28:31], v[52:55], v[132:135]\n"
  "ds_read_b64_tr_b16 v[46:47], v6 offset:26624\n"
  "ds_read_b128 v[56:59], v12 offset:18432\n"
  "ds_read_b128 v[60:63], v12 offset:19456\n"
  "s_waitcnt lgkmcnt(8)\n"
  "v_mfma_f32_16x16x32_bf16 v[140:143], v[32:35], v[52:55], v[140:143]\n"
  "ds_read_b64_tr_b16 v[28:29], v7 offset:24576\n"
  "v_mfma_f32_16x16x32_bf16 v[136:139], v[32:35], v[48:51], v[136:139]\n"
  "ds_read_b64_tr_b16 v[30:31], v7 offset:26624\n"
  "s_waitcnt lgkmcnt(8)\n"
  "v_mfma_f32_16x16x32_bf16 v[144:147], v[36:39], v[48:51], v[144:147]\n"
  "ds_read_b64_tr_b16 v[32:33], v8 offset:24576\n"
  "v_mfma_f32_16x16x32_bf16 v[148:151], v[36:39], v[52:55], v[148:151]\n"
  "ds_read_b64_tr_b16 v[34:35], v8 offset:26624\n"
  "s_waitcnt lgkmcnt(8)\n"
  "v_mfma_f32_16x16x32_bf16 v[156:159], v[40:43], v[52:55], v[156:159]\n"
  "ds_read_b64_tr_b16 v[36:37], v9 offset:24576\n"
  "v_mfma_f32_16x16x32_bf16 v[152:155], v[40:43], v[48:51], v[152:155]\n"
  "ds_read_b64_tr_b16 v[38:39], v9 offset:26624\n"
  "s_waitcnt lgkmcnt(8)\n"
  "v_mfma_f32_16x16x32_bf16 v[160:163], v[44:47], v[48:51], v[160:163]\n"
  "ds_read_b64_tr_b16 v[40:41], v6 offset:20480\n"
  "v_mfma_f32_16x16x32_bf16 v[164:167], v[44:47], v[52:55], v[164:167]\n"
  "ds_read_b64_tr_b16 v[42:43], v6 offset:22528\n"
  "s_waitcnt lgkmcnt(6)\n"
  "v_mfma_f32_16x16x32_bf16 v[172:175], v[28:31], v[52:55], v[172:175]\n"
  "ds_read_b64_tr_b16 v[44:45], v7 offset:20480\n"
  "v_mfma_f32_16x16x32_bf16 v[168:171], v[28:31], v[48:51], v[168:171]\n"
  "ds_read_b64_tr_b16 v[46:47], v7 offset:22528\n"
  "s_waitcnt lgkmcnt(6)\n"
  "v_mfma_f32_16x16x32_bf16 v[176:179], v[32:35], v[48:51], v[176:179]\n"
  "ds_read_b64_tr_b16 v[28:29], v8 offset:20480\n"
  "v_mfma_f32_16x16x32_bf16 v[180:183], v[32:35], v[52:55], v[180:183]\n"
  "ds_read_b64_tr_b16 v[30:31], v8 offset:22528\n"
  "s_waitcnt lgkmcnt(6)\n"
  "v_mfma_f32_16x16x32_bf16 v[188:191], v[36:39], v[52:55], v[188:191]\n"
  "ds_read_b64_tr_b16 v[32:33], v9 offset:20480\n"
  "v_mfma_f32_16x16x32_bf16 v[184:187], v[36:39], v[48:51], v[184:187]\n"
  "ds_read_b64_tr_b16 v[34:35], v9 offset:22528\n"
  "s_waitcnt lgkmcnt(6)\n"
  "v_mfma_f32_16x16x32_bf16 v[128:131], v[40:43], v[56:59], v[128:131]\n"
  "ds_read_b64_tr_b16 v[36:37], v6 offset:28672\n"
  "v_mfma_f32_16x16x32_bf16 v[132:135], v[40:43], v[60:63], v[132:135]\n"
  "ds_read_b64_tr_b16 v[38:39], v6 offset:30720\n"
  "s_waitcnt lgkmcnt(6)\n"
  "v_mfma_f32_16x16x32_bf16 v[140:143], v[44:47], v[60:63], v[140:143]\n"
  "ds_read_b64_tr_b16 v[40:41], v7 offset:28672\n"
  "v_mfma_f32_16x16x32_bf16 v[136:139], v[44:47], v[56:59], v[136:139]\n"
  "ds_read_b64_tr_b16 v[42:43], v7 offset:30720\n"
  "s_waitcnt lgkmcnt(6)\n"
  "v_mfma_f32_16x16x32_bf16 v[144:147], v[28:31], v[56:59], v[144:147]\n"
  "ds_read_b64_tr_b16 v[44:45], v8 offset:28672\n"
  "v_mfma_f32_16x16x32_bf16 v[148:151], v[28:31], v[60:63], v[148:151]\n"
  "ds_read_b64_tr_b16 v[46:47], v8 offset:30720\n"
  "s_waitcnt lgkmcnt(6)\n"
  "v_mfma_f32_16x16x32_bf16 v[156:159], v[32:35], v[60:63], v[156:159]\n"
  "ds_read_b64_tr_b16 v[28:29], v9 offset:28672\n"
  "v_mfma_f32_16x16x32_bf16 v[152:155], v[32:35], v[56:59], v[152:155]\n"
  "ds_read_b64_tr_b16 v[30:31], v9 offset:30720\n"
  "s_waitcnt lgkmcnt(6)\n"
  "v_mfma_f32_16x16x32_bf16 v[160:163], v[36:39], v[56:59], v[160:163]\n"
  "s_barrier\n"
  "v_add_u32 v6, 131072, v6\n"
  "v_add_u32 v7, 131072, v7\n"
  "v_add_u32 v8, 131072, v8\n"
  "v_add_u32 v9, 131072, v9\n"
  "ds_read_b64_tr_b16 v[32:33], v6 offset:0\n"
  "v_mfma_f32_16x16x32_bf16 v[164:167], v[36:39], v[60:63], v[164:167]\n"
  "ds_read_b64_tr_b16 v[34:35], v6 offset:2048\n"
  "ds_read_b128 v[48:51], v12 offset:32768\n"
  "ds_read_b128 v[52:55], v12 offset:33792\n"
  "s_waitcnt lgkmcnt(8)\n"
  "v_mfma_f32_16x16x32_bf16 v[172:175], v[40:43], v[60:63], v[172:175]\n"
  "ds_read_b64_tr_b16 v[36:37], v7 offset:0\n"
  "v_mfma_f32_16x16x32_bf16 v[168:171], v[40:43], v[56:59], v[168:171]\n"
  "ds_read_b64_tr_b16 v[38:39], v7 offset:2048\n"
  "s_waitcnt lgkmcnt(8)\n"
  "v_mfma_f32_16x16x32_bf16 v[176:179], v[44:47], v[56:59], v[176:179]\n"
  "ds_read_b64_tr_b16 v[40:41], v8 offset:0\n"
  "v_mfma_f32_16x16x32_bf16 v[180:183], v[44:47], v[60:63], v[180:183]\n"
  "ds_read_b64_tr_b16 v[42:43], v8 offset:2048\n"
  "s_waitcnt lgkmcnt(8)\n"
  "v_mfma_f32_16x16x32_bf16 v[188:191], v[28:31], v[60:63], v[188:191]\n"
  "ds_read_b64_tr_b16 v[44:45], v9 offset:0\n"
  "v_mfma_f32_16x16x32_bf16 v[184:187], v[28:31], v[56:59], v[184:187]\n"
  "ds_read_b64_tr_b16 v[46:47], v9 offset:2048\n"
  "s_waitcnt lgkmcnt(7)\n"
  "v_mfma_f32_16x16x32_bf16 v[128:131], v[32:35], v[48:51], v[128:131]\n"
  "ds_read_b64_tr_b16 v[28:29], v6 offset:8192\n"
  "s_waitcnt lgkmcnt(7)\n"
  "v_mfma_f32_16x16x32_bf16 v[132:135], v[32:35], v[52:55], v[132:135]\n"
  "ds_read_b64_tr_b16 v[30:31], v6 offset:10240\n"
  "ds_read_b128 v[56:59], v12 offset:34816\n"
  "ds_read_b128 v[60:63], v12 offset:35840\n"
  "s_waitcnt lgkmcnt(8)\n"
  "v_mfma_f32_16x16x32_bf16 v[140:143], v[36:39], v[52:55], v[140:143]\n"
  "ds_read_b64_tr_b16 v[32:33], v7 offset:8192\n"
  "v_mfma_f32_16x16x32_bf16 v[136:139], v[36:39], v[48:51], v[136:139]\n"
  "ds_read_b64_tr_b16 v[34:35], v7 offset:10240\n"
  "s_waitcnt lgkmcnt(8)\n"
  "v_mfma_f32_16x16x32_bf16 v[144:147], v[40:43], v[48:51], v[144:147]\n"
  "ds_read_b64_tr_b16 v[36:37], v8 offset:8192\n"
  "v_mfma_f32_16x16x32_bf16 v[148:151], v[40:43], v[52:55], v[148:151]\n"
  "ds_read_b64_tr_b16 v[38:39], v8 offset:10240\n"
  "s_waitcnt lgkmcnt(8)\n"
  "v_mfma_f32_16x16x32_bf16 v[156:159], v[44:47], v[52:55], v[156:159]\n"
  "ds_read_b64_tr_b16 v[40:41], v9 offset:8192\n"
  "v_mfma_f32_16x16x32_bf16 v[152:155], v[44:47], v[48:51], v[152:155]\n"
  "ds_read_b64_tr_b16 v[42:43], v9 offset:10240\n"
  "s_waitcnt lgkmcnt(8)\n"
  "v_mfma_f32_16x16x32_bf16 v[160:163], v[28:31], v[48:51], v[160:163]\n"
  "ds_read_b64_tr_b16 v[44:45], v6 offset:4096\n"
  "v_mfma_f32_16x16x32_bf16 v[164:167], v[28:31], v[52:55], v[164:167]\n"
  "ds_read_b64_tr_b16 v[46:47], v6 offset:6144\n"
  "s_waitcnt lgkmcnt(6)\n"
  "v_mfma_f32_16x16x32_bf16 v[172:175], v[32:35], v[52:55], v[172:175]\n"
  "ds_read_b64_tr_b16 v[28:29], v7 offset:4096\n"
  "v_mfma_f32_16x16x32_bf16 v[168:171], v[32:35], v[48:51], v[168:171]\n"
  "ds_read_b64_tr_b16 v[30:31], v7 offset:6144\n"
  "s_waitcnt lgkmcnt(6)\n"
  "v_mfma_f32_16x16x32_bf16 v[176:179], v[36:39], v[48:51], v[176:179]\n"
  "ds_read_b64_tr_b16 v[32:33], v8 offset:4096\n"
  "v_mfma_f32_16x16x32_bf16 v[180:183], v[36:39], v[52:55], v[180:183]\n"
  "ds_read_b64_tr_b16 v[34:35], v8 offset:6144\n"
  "s_waitcnt lgkmcnt(6)\n"
  "v_mfma_f32_16x16x32_bf16 v[188:191], v[40:43], v[52:55], v[188:191]\n"
  "ds_read_b64_tr_b16 v[36:37], v9 offset:4096\n"
  "v_mfma_f32_16x16x32_bf16 v[184:187], v[40:43], v[48:51], v[184:187]\n"
  "ds_read_b64_tr_b16 v[38:39], v9 offset:6144\n"
  "s_waitcnt lgkmcnt(6)\n"
  "v_mfma_f32_16x16x32_bf16 v[128:131], v[44:47], v[56:59], v[128:131]\n"
  "ds_read_b64_tr_b16 v[40:41], v6 offset:12288\n"
  "v_mfma_f32_16x16x32_bf16 v[132:135], v[44:47], v[60:63], v[132:135]\n"
  "ds_read_b64_tr_b16 v[42:43], v6 offset:14336\n"
  "s_waitcnt lgkmcnt(6)\n"
  "v_mfma_f32_16x16x32_bf16 v[140:143], v[28:31], v[60:63], v[140:143]\n"
  "ds_read_b64_tr_b16 v[44:45], v7 offset:12288\n"
  "v_mfma_f32_16x16x32_bf16 v[136:139], v[28:31], v[56:59], v[136:139]\n"
  "ds_read_b64_tr_b16 v[46:47], v7 offset:14336\n"
  "s_waitcnt lgkmcnt(6)\n"
  "v_mfma_f32_16x16x32_bf16 v[144:147], v[32:35], v[56:59], v[144:147]\n"
  "ds_read_b64_tr_b16 v[28:29], v8 offset:12288\n"
  "v_mfma_f32_16x16x32_bf16 v[148:151], v[32:35], v[60:63], v[148:151]\n"
  "ds_read_b64_tr_b16 v[30:31], v8 offset:14336\n"
  "s_waitcnt lgkmcnt(6)\n"
  "v_mfma_f32_16x16x32_bf16 v[156:159], v[36:39], v[60:63], v[156:159]\n"
  "ds_read_b64_tr_b16 v[32:33], v9 offset:12288\n"
  "v_mfma_f32_16x16x32_bf16 v[152:155], v[36:39], v[56:59], v[152:155]\n"
  "ds_read_b64_tr_b16 v[34:35], v9 offset:14336\n"
  "s_waitcnt lgkmcnt(6)\n"
  "v_mfma_f32_16x16x32_bf16 v[160:163], v[40:43], v[56:59], v[160:163]\n"
  "s_barrier\n"
  "ds_read_b64_tr_b16 v[36:37], v6 offset:16384\n"
  "v_mfma_f32_16x16x32_bf16 v[164:167], v[40:43], v[60:63], v[164:167]\n"
  "ds_read_b64_tr_b16 v[38:39], v6 offset:18432\n"
  "ds_read_b128 v[48:51], v12 offset:49152\n"
  "ds_read_b128 v[52:55], v12 offset:50176\n"
  "s_waitcnt lgkmcnt(8)\n"
  "v_mfma_f32_16x16x32_bf16 v[172:175], v[44:47], v[60:63], v[172:175]\n"
  "ds_read_b64_tr_b16 v[40:41], v7 offset:16384\n"
  "v_mfma_f32_16x16x32_bf16 v[168:171], v[44:47], v[56:59], v[168:171]\n"
  "ds_read_b64_tr_b16 v[42:43], v7 offset:18432\n"
  "s_waitcnt lgkmcnt(8)\n"
  "v_mfma_f32_16x16x32_bf16 v[176:179], v[28:31], v[56:59], v[176:179]\n"
  "ds_read_b64_tr_b16 v[44:45], v8 offset:16384\n"
  "v_mfma_f32_16x16x32_bf16 v[180:183], v[28:31], v[60:63], v[180:183]\n"
  "ds_read_b64_tr_b16 v[46:47], v8 offset:18432\n"
  "s_waitcnt lgkmcnt(8)\n"
  "v_mfma_f32_16x16x32_bf16 v[188:191], v[32:35], v[60:63], v[188:191]\n"
  "ds_read_b64_tr_b16 v[28:29], v9 offset:16384\n"
  "v_mfma_f32_16x16x32_bf16 v[184:187], v[32:35], v[56:59], v[184:187]\n"
  "ds_read_b64_tr_b16 v[30:31], v9 offset:18432\n"
  "s_waitcnt lgkmcnt(7)\n"
  "v_mfma_f32_16x16x32_bf16 v[128:131], v[36:39], v[48:51], v[128:131]\n"
  "ds_read_b64_tr_b16 v[32:33], v6 offset:24576\n"
  "s_waitcnt lgkmcnt(7)\n"
  "v_mfma_f32_16x16x32_bf16 v[132:135], v[36:39], v[52:55], v[132:135]\n"
  "ds_read_b64_tr_b16 v[34:35], v6 offset:26624\n"
  "ds_read_b128 v[56:59], v12 offset:51200\n"
  "ds_read_b128 v[60:63], v12 offset:52224\n"
  "s_waitcnt lgkmcnt(8)\n"
  "v_mfma_f32_16x16x32_bf16 v[140:143], v[40:43], v[52:55], v[140:143]\n"
  "ds_read_b64_tr_b16 v[36:37], v7 offset:24576\n"
  "v_mfma_f32_16x16x32_bf16 v[136:139], v[40:43], v[48:51], v[136:139]\n"
  "ds_read_b64_tr_b16 v[38:39], v7 offset:26624\n"
  "s_waitcnt lgkmcnt(8)\n"
  "v_mfma_f32_16x16x32_bf16 v[144:147], v[44:47], v[48:51], v[144:147]\n"
  "ds_read_b64_tr_b16 v[40:41], v8 offset:24576\n"
  "v_mfma_f32_16x16x32_bf16 v[148:151], v[44:47], v[52:55], v[148:151]\n"
  "ds_read_b64_tr_b16 v[42:43], v8 offset:26624\n"
  "s_waitcnt lgkmcnt(8)\n"
  "v_mfma_f32_16x16x32_bf16 v[156:159], v[28:31], v[52:55], v[156:159]\n"
  "ds_read_b64_tr_b16 v[44:45], v9 offset:24576\n"
  "v_mfma_f32_16x16x32_bf16 v[152:155], v[28:31], v[48:51], v[152:155]\n"
  "ds_read_b64_tr_b16 v[46:47], v9 offset:26624\n"
  "s_waitcnt lgkmcnt(8)\n"
  "v_mfma_f32_16x16x32_bf16 v[160:163], v[32:35], v[48:51], v[160:163]\n"
  "ds_read_b64_tr_b16 v[28:29], v6 offset:20480\n"
  "v_mfma_f32_16x16x32_bf16 v[164:167], v[32:35], v[52:55], v[164:167]\n"
  "ds_read_b64_tr_b16 v[30:31], v6 offset:22528\n"
  "s_waitcnt lgkmcnt(6)\n"
  "v_mfma_f32_16x16x32_bf16 v[172:175], v[36:39], v[52:55], v[172:175]\n"
  "ds_read_b64_tr_b16 v[32:33], v7 offset:20480\n"
  "v_mfma_f32_16x16x32_bf16 v[168:171], v[36:39], v[48:51], v[168:171]\n"
  "ds_read_b64_tr_b16 v[34:35], v7 offset:22528\n"
  "s_waitcnt lgkmcnt(6)\n"
  "v_mfma_f32_16x16x32_bf16 v[176:179], v[40:43], v[48:51], v[176:179]\n"
  "ds_read_b64_tr_b16 v[36:37], v8 offset:20480\n"
  "v_mfma_f32_16x16x32_bf16 v[180:183], v[40:43], v[52:55], v[180:183]\n"
  "ds_read_b64_tr_b16 v[38:39], v8 offset:22528\n"
  "s_waitcnt lgkmcnt(6)\n"
  "v_mfma_f32_16x16x32_bf16 v[188:191], v[44:47], v[52:55], v[188:191]\n"
  "ds_read_b64_tr_b16 v[40:41], v9 offset:20480\n"
  "v_mfma_f32_16x16x32_bf16 v[184:187], v[44:47], v[48:51], v[184:187]\n"
  "ds_read_b64_tr_b16 v[42:43], v9 offset:22528\n"
  "s_waitcnt lgkmcnt(6)\n"
  "v_mfma_f32_16x16x32_bf16 v[128:131], v[28:31], v[56:59], v[128:131]\n"
  "ds_read_b64_tr_b16 v[44:45], v6 offset:28672\n"
  "v_mfma_f32_16x16x32_bf16 v[132:135], v[28:31], v[60:63], v[132:135]\n"
  "ds_read_b64_tr_b16 v[46:47], v6 offset:30720\n"
  "s_waitcnt lgkmcnt(6)\n"
  "v_mfma_f32_16x16x32_bf16 v[140:143], v[32:35], v[60:63], v[140:143]\n"
  "ds_read_b64_tr_b16 v[28:29], v7 offset:28672\n"
  "v_mfma_f32_16x16x32_bf16 v[136:139], v[32:35], v[56:59], v[136:139]\n"
  "ds_read_b64_tr_b16 v[30:31], v7 offset:30720\n"
  "s_waitcnt lgkmcnt(6)\n"
  "v_mfma_f32_16x16x32_bf16 v[144:147], v[36:39], v[56:59], v[144:147]\n"
  "ds_read_b64_tr_b16 v[32:33], v8 offset:28672\n"
  "v_mfma_f32_16x16x32_bf16 v[148:151], v[36:39], v[60:63], v[148:151]\n"
  "ds_read_b64_tr_b16 v[34:35], v8 offset:30720\n"
  "s_waitcnt lgkmcnt(6)\n"
  "v_mfma_f32_16x16x32_bf16 v[156:159], v[40:43], v[60:63], v[156:159]\n"
  "ds_read_b64_tr_b16 v[36:37], v9 offset:28672\n"
  "v_mfma_f32_16x16x32_bf16 v[152:155], v[40:43], v[56:59], v[152:155]\n"
  "ds_read_b64_tr_b16 v[38:39], v9 offset:30720\n"
  "s_waitcnt lgkmcnt(6)\n"
  "v_mfma_f32_16x16x32_bf16 v[160:163], v[44:47], v[56:59], v[160:163]\n"
  "s_barrier\n"
  "v_add_u32 v6, 4294836224, v6\n"
  "v_add_u32 v7, 4294836224, v7\n"
  "v_add_u32 v8, 4294836224, v8\n"
  "v_add_u32 v9, 4294836224, v9\n"
  "ds_read_b64_tr_b16 v[40:41], v6 offset:32768\n"
  "v_mfma_f32_16x16x32_bf16 v[164:167], v[44:47], v[60:63], v[164:167]\n"
  "ds_read_b64_tr_b16 v[42:43], v6 offset:34816\n"
  "ds_read_b128 v[48:51], v12 offset:0\n"
  "ds_read_b128 v[52:55], v12 offset:1024\n"
  "s_waitcnt lgkmcnt(8)\n"
  "v_mfma_f32_16x16x32_bf16 v[172:175], v[28:31], v[60:63], v[172:175]\n"
  "ds_read_b64_tr_b16 v[44:45], v7 offset:32768\n"
  "v_mfma_f32_16x16x32_bf16 v[168:171], v[28:31], v[56:59], v[168:171]\n"
  "ds_read_b64_tr_b16 v[46:47], v7 offset:34816\n"
  "s_waitcnt lgkmcnt(8)\n"
  "v_mfma_f32_16x16x32_bf16 v[176:179], v[32:35], v[56:59], v[176:179]\n"
  "ds_read_b64_tr_b16 v[28:29], v8 offset:32768\n"
  "v_mfma_f32_16x16x32_bf16 v[180:183], v[32:35], v[60:63], v[180:183]\n"
  "ds_read_b64_tr_b16 v[30:31], v8 offset:34816\n"
  "s_waitcnt lgkmcnt(8)\n"
  "v_mfma_f32_16x16x32_bf16 v[188:191], v[36:39], v[60:63], v[188:191]\n"
  "ds_read_b64_tr_b16 v[32:33], v9 offset:32768\n"
  "v_mfma_f32_16x16x32_bf16 v[184:187], v[36:39], v[56:59], v[184:187]\n"
  "ds_read_b64_tr_b16 v[34:35], v9 offset:34816\n"
  "s_waitcnt lgkmcnt(7)\n"
  "v_mfma_f32_16x16x32_bf16 v[128:131], v[40:43], v[48:51], v[128:131]\n"
  "ds_read_b64_tr_b16 v[36:37], v6 offset:40960\n"
  "s_waitcnt lgkmcnt(7)\n"
  "v_mfma_f32_16x16x32_bf16 v[132:135], v[40:43], v[52:55], v[132:135]\n"
  "ds_read_b64_tr_b16 v[38:39], v6 offset:43008\n"
  "ds_read_b128 v[56:59], v12 offset:2048\n"
  "ds_read_b128 v[60:63], v12 offset:3072\n"
  "s_waitcnt lgkmcnt(8)\n"
  "v_mfma_f32_16x16x32_bf16 v[140:143], v[44:47], v[52:55], v[140:143]\n"
  "ds_read_b64_tr_b16 v[40:41], v7 offset:40960\n"
  "v_mfma_f32_16x16x32_bf16 v[136:139], v[44:47], v[48:51], v[136:139]\n"
  "ds_read_b64_tr_b16 v[42:43], v7 offset:43008\n"
  "s_waitcnt lgkmcnt(8)\n"
  "v_mfma_f32_16x16x32_bf16 v[144:147], v[28:31], v[48:51], v[144:147]\n"
  "ds_read_b64_tr_b16 v[44:45], v8 offset:40960\n"
  "v_mfma_f32_16x16x32_bf16 v[148:151], v[28:31], v[52:55], v[148:151]\n"
  "ds_read_b64_tr_b16 v[46:47], v8 offset:43008\n"
  "s_waitcnt lgkmcnt(8)\n"
  "v_mfma_f32_16x16x32_bf16 v[156:159], v[32:35], v[52:55], v[156:159]\n"
  "ds_read_b64_tr_b16 v[28:29], v9 offset:40960\n"
  "v_mfma_f32_16x16x32_bf16 v[152:155], v[32:35], v[48:51], v[152:155]\n"
  "ds_read_b64_tr_b16 v[30:31], v9 offset:43008\n"
  "s_waitcnt lgkmcnt(8)\n"
  "v_mfma_f32_16x16x32_bf16 v[160:163], v[36:39], v[48:51], v[160:163]\n"
  "ds_read_b64_tr_b16 v[32:33], v6 offset:36864\n"
  "v_mfma_f32_16x16x32_bf16 v[164:167], v[36:39], v[52:55], v[164:167]\n"
  "ds_read_b64_tr_b16 v[34:35], v6 offset:38912\n"
  "s_waitcnt lgkmcnt(6)\n"
  "v_mfma_f32_16x16x32_bf16 v[172:175], v[40:43], v[52:55], v[172:175]\n"
  "ds_read_b64_tr_b16 v[36:37], v7 offset:36864\n"
  "v_mfma_f32_16x16x32_bf16 v[168:171], v[40:43], v[48:51], v[168:171]\n"
  "ds_read_b64_tr_b16 v[38:39], v7 offset:38912\n"
  "s_waitcnt lgkmcnt(6)\n"
  "v_mfma_f32_16x16x32_bf16 v[176:179], v[44:47], v[48:51], v[176:179]\n"
  "ds_read_b64_tr_b16 v[40:41], v8 offset:36864\n"
  "v_mfma_f32_16x16x32_bf16 v[180:183], v[44:47], v[52:55], v[180:183]\n"
  "ds_read_b64_tr_b16 v[42:43], v8 offset:38912\n"
  "s_waitcnt lgkmcnt(6)\n"
  "v_mfma_f32_16x16x32_bf16 v[188:191], v[28:31], v[52:55], v[188:191]\n"
  "ds_read_b64_tr_b16 v[44:45], v9 offset:36864\n"
  "v_mfma_f32_16x16x32_bf16 v[184:187], v[28:31], v[48:51], v[184:187]\n"
  "ds_read_b64_tr_b16 v[46:47], v9 offset:38912\n"
  "s_waitcnt lgkmcnt(6)\n"
  "v_mfma_f32_16x16x32_bf16 v[128:131], v[32:35], v[56:59], v[128:131]\n"
  "ds_read_b64_tr_b16 v[28:29], v6 offset:45056\n"
  "v_mfma_f32_16x16x32_bf16 v[132:135], v[32:35], v[60:63], v[132:135]\n"
  "ds_read_b64_tr_b16 v[30:31], v6 offset:47104\n"
  "s_waitcnt lgkmcnt(6)\n"
  "v_mfma_f32_16x16x32_bf16 v[140:143], v[36:39], v[60:63], v[140:143]\n"
  "ds_read_b64_tr_b16 v[32:33], v7 offset:45056\n"
  "v_mfma_f32_16x16x32_bf16 v[136:139], v[36:39], v[56:59], v[136:139]\n"
  "ds_read_b64_tr_b16 v[34:35], v7 offset:47104\n"
  "s_waitcnt lgkmcnt(6)\n"
  "v_mfma_f32_16x16x32_bf16 v[144:147], v[40:43], v[56:59], v[144:147]\n"
  "ds_read_b64_tr_b16 v[36:37], v8 offset:45056\n"
  "v_mfma_f32_16x16x32_bf16 v[148:151], v[40:43], v[60:63], v[148:151]\n"
  "ds_read_b64_tr_b16 v[38:39], v8 offset:47104\n"
  "s_waitcnt lgkmcnt(6)\n"
  "v_mfma_f32_16x16x32_bf16 v[156:159], v[44:47], v[60:63], v[156:159]\n"
  "ds_read_b64_tr_b16 v[40:41], v9 offset:45056\n"
  "v_mfma_f32_16x16x32_bf16 v[152:155], v[44:47], v[56:59], v[152:155]\n"
  "ds_read_b64_tr_b16 v[42:43], v9 offset:47104\n"
  "s_waitcnt lgkmcnt(6)\n"
  "v_mfma_f32_16x16x32_bf16 v[160:163], v[28:31], v[56:59], v[160:163]\n"
  "s_barrier\n"
  "ds_read_b64_tr_b16 v[44:45], v6 offset:49152\n"
  "v_mfma_f32_16x16x32_bf16 v[164:167], v[28:31], v[60:63], v[164:167]\n"
  "ds_read_b64_tr_b16 v[46:47], v6 offset:51200\n"
  "ds_read_b128 v[48:51], v12 offset:16384\n"
  "ds_read_b128 v[52:55], v12 offset:17408\n"
  "s_waitcnt lgkmcnt(8)\n"
  "v_mfma_f32_16x16x32_bf16 v[172:175], v[32:35], v[60:63], v[172:175]\n"
  "ds_read_b64_tr_b16 v[28:29], v7 offset:49152\n"
  "v_mfma_f32_16x16x32_bf16 v[168:171], v[32:35], v[56:59], v[168:171]\n"
  "ds_read_b64_tr_b16 v[30:31], v7 offset:51200\n"
  "s_waitcnt lgkmcnt(8)\n"
  "v_mfma_f32_16x16x32_bf16 v[176:179], v[36:39], v[56:59], v[176:179]\n"
  "ds_read_b64_tr_b16 v[32:33], v8 offset:49152\n"
  "v_mfma_f32_16x16x32_bf16 v[180:183], v[36:39], v[60:63], v[180:183]\n"
  "ds_read_b64_tr_b16 v[34:35], v8 offset:51200\n"
  "s_waitcnt lgkmcnt(8)\n"
  "v_mfma_f32_16x16x32_bf16 v[188:191], v[40:43], v[60:63], v[188:191]\n"
  "ds_read_b64_tr_b16 v[36:37], v9 offset:49152\n"
  "v_mfma_f32_16x16x32_bf16 v[184:187], v[40:43], v[56:59], v[184:187]\n"
  "ds_read_b64_tr_b16 v[38:39], v9 offset:51200\n"
  "s_waitcnt lgkmcnt(7)\n"
  "v_mfma_f32_16x16x32_bf16 v[128:131], v[44:47], v[48:51], v[128:131]\n"
  "ds_read_b64_tr_b16 v[40:41], v6 offset:57344\n"
  "s_waitcnt lgkmcnt(7)\n"
  "v_mfma_f32_16x16x32_bf16 v[132:135], v[44:47], v[52:55], v[132:135]\n"
  "ds_read_b64_tr_b16 v[42:43], v6 offset:59392\n"
  "ds_read_b128 v[56:59], v12 offset:18432\n"
  "ds_read_b128 v[60:63], v12 offset:19456\n"
  "v_mov_b32 v26, s48\n"
  "v_mov_b32 v27, s49\n"
  "v_mov_b32 v22, s52\n"
  "v_mov_b32 v23, s53\n"
  "v_min_u32 v13, 2, v0\n"
  "v_xor_b32 v13, 3, v13\n"
  "v_xor_b32 v13, s92, v13\n"
  "v_lshlrev_b32 v13, 9, v13\n"
  "s_waitcnt lgkmcnt(8)\n"
  "v_mfma_f32_16x16x32_bf16 v[140:143], v[28:31], v[52:55], v[140:143]\n"
  "ds_read_b64_tr_b16 v[44:45], v7 offset:57344\n"
  "v_mfma_f32_16x16x32_bf16 v[136:139], v[28:31], v[48:51], v[136:139]\n"
  "ds_read_b64_tr_b16 v[46:47], v7 offset:59392\n"
  "s_waitcnt lgkmcnt(8)\n"
  "v_mfma_f32_16x16x32_bf16 v[144:147], v[32:35], v[48:51], v[144:147]\n"
  "ds_read_b64_tr_b16 v[28:29], v8 offset:57344\n"
  "v_mfma_f32_16x16x32_bf16 v[148:151], v[32:35], v[52:55], v[148:151]\n"
  "ds_read_b64_tr_b16 v[30:31], v8 offset:59392\n"
  "s_waitcnt lgkmcnt(8)\n"
  "v_mfma_f32_16x16x32_bf16 v[156:159], v[36:39], v[52:55], v[156:159]\n"
  "ds_read_b64_tr_b16 v[32:33], v9 offset:57344\n"
  "v_mfma_f32_16x16x32_bf16 v[152:155], v[36:39], v[48:51], v[152:155]\n"
  "ds_read_b64_tr_b16 v[34:35], v9 offset:59392\n"
  "s_waitcnt lgkmcnt(8)\n"
  "v_mfma_f32_16x16x32_bf16 v[160:163], v[40:43], v[48:51], v[160:163]\n"
  "ds_read_b64_tr_b16 v[36:37], v6 offset:53248\n"
  "v_mfma_f32_16x16x32_bf16 v[164:167], v[40:43], v[52:55], v[164:167]\n"
  "ds_read_b64_tr_b16 v[38:39], v6 offset:55296\n"
  "s_waitcnt lgkmcnt(6)\n"
  "v_mfma_f32_16x16x32_bf16 v[172:175], v[44:47], v[52:55], v[172:175]\n"
  "ds_read_b64_tr_b16 v[40:41], v7 offset:53248\n"
  "v_mfma_f32_16x16x32_bf16 v[168:171], v[44:47], v[48:51], v[168:171]\n"
  "ds_read_b64_tr_b16 v[42:43], v7 offset:55296\n"
  "s_waitcnt lgkmcnt(6)\n"
  "v_mfma_f32_16x16x32_bf16 v[176:179], v[28:31], v[48:51], v[176:179]\n"
  "ds_read_b64_tr_b16 v[44:45], v8 offset:53248\n"
  "v_mfma_f32_16x16x32_bf16 v[180:183], v[28:31], v[52:55], v[180:183]\n"
  "ds_read_b64_tr_b16 v[46:47], v8 offset:55296\n"
  "s_waitcnt lgkmcnt(6)\n"
  "v_mfma_f32_16x16x32_bf16 v[188:191], v[32:35], v[52:55], v[188:191]\n"
  "ds_read_b64_tr_b16 v[28:29], v9 offset:53248\n"
  "v_mfma_f32_16x16x32_bf16 v[184:187], v[32:35], v[48:51], v[184:187]\n"
  "ds_read_b64_tr_b16 v[30:31], v9 offset:55296\n"
  "s_waitcnt lgkmcnt(6)\n"
  "v_mfma_f32_16x16x32_bf16 v[128:131], v[36:39], v[56:59], v[128:131]\n"
  "ds_read_b64_tr_b16 v[32:33], v6 offset:61440\n"
  "v_mfma_f32_16x16x32_bf16 v[132:135], v[36:39], v[60:63], v[132:135]\n"
  "ds_read_b64_tr_b16 v[34:35], v6 offset:63488\n"
  "s_waitcnt lgkmcnt(6)\n"
  "v_mfma_f32_16x16x32_bf16 v[140:143], v[40:43], v[60:63], v[140:143]\n"
  "ds_read_b64_tr_b16 v[36:37], v7 offset:61440\n"
  "v_mfma_f32_16x16x32_bf16 v[136:139], v[40:43], v[56:59], v[136:139]\n"
  "ds_read_b64_tr_b16 v[38:39], v7 offset:63488\n"
  "s_waitcnt lgkmcnt(6)\n"
  "v_mfma_f32_16x16x32_bf16 v[144:147], v[44:47], v[56:59], v[144:147]\n"
  "ds_read_b64_tr_b16 v[40:41], v8 offset:61440\n"
  "v_mfma_f32_16x16x32_bf16 v[148:151], v[44:47], v[60:63], v[148:151]\n"
  "ds_read_b64_tr_b16 v[42:43], v8 offset:63488\n"
  "s_waitcnt lgkmcnt(6)\n"
  "v_mfma_f32_16x16x32_bf16 v[156:159], v[28:31], v[60:63], v[156:159]\n"
  "ds_read_b64_tr_b16 v[44:45], v9 offset:61440\n"
  "v_mfma_f32_16x16x32_bf16 v[152:155], v[28:31], v[56:59], v[152:155]\n"
  "ds_read_b64_tr_b16 v[46:47], v9 offset:63488\n"
  "s_waitcnt lgkmcnt(6)\n"
  "v_mfma_f32_16x16x32_bf16 v[160:163], v[32:35], v[56:59], v[160:163]\n"
  "s_barrier\n"
  "ds_read_b64_tr_b16 v[28:29], v6 offset:0\n"
  "v_mfma_f32_16x16x32_bf16 v[164:167], v[32:35], v[60:63], v[164:167]\n"
  "ds_read_b64_tr_b16 v[30:31], v6 offset:2048\n"
  "ds_read_b128 v[48:51], v12 offset:32768\n"
  "ds_read_b128 v[52:55], v12 offset:33792\n"
  "s_waitcnt lgkmcnt(8)\n"
  "v_mfma_f32_16x16x32_bf16 v[172:175], v[36:39], v[60:63], v[172:175]\n"
  "ds_read_b64_tr_b16 v[32:33], v7 offset:0\n"
  "v_mfma_f32_16x16x32_bf16 v[168:171], v[36:39], v[56:59], v[168:171]\n"
  "ds_read_b64_tr_b16 v[34:35], v7 offset:2048\n"
  "s_waitcnt lgkmcnt(8)\n"
  "v_mfma_f32_16x16x32_bf16 v[176:179], v[40:43], v[56:59], v[176:179]\n"
  "ds_read_b64_tr_b16 v[36:37], v8 offset:0\n"
  "v_mfma_f32_16x16x32_bf16 v[180:183], v[40:43], v[60:63], v[180:183]\n"
  "ds_read_b64_tr_b16 v[38:39], v8 offset:2048\n"
  "s_waitcnt lgkmcnt(8)\n"
  "v_mfma_f32_16x16x32_bf16 v[188:191], v[44:47], v[60:63], v[188:191]\n"
  "ds_read_b64_tr_b16 v[40:41], v9 offset:0\n"
  "v_mfma_f32_16x16x32_bf16 v[184:187], v[44:47], v[56:59], v[184:187]\n"
  "ds_read_b64_tr_b16 v[42:43], v9 offset:2048\n"
  "s_waitcnt lgkmcnt(7)\n"
  "v_mfma_f32_16x16x32_bf16 v[128:131], v[28:31], v[48:51], v[128:131]\n"
  "ds_read_b64_tr_b16 v[44:45], v6 offset:8192\n"
  "s_waitcnt lgkmcnt(7)\n"
  "v_mfma_f32_16x16x32_bf16 v[132:135], v[28:31], v[52:55], v[132:135]\n"
  "ds_read_b64_tr_b16 v[46:47], v6 offset:10240\n"
  "ds_read_b128 v[56:59], v12 offset:34816\n"
  "ds_read_b128 v[60:63], v12 offset:35840\n"
  "s_waitcnt lgkmcnt(8)\n"
  "v_mfma_f32_16x16x32_bf16 v[140:143], v[32:35], v[52:55], v[140:143]\n"
  "ds_read_b64_tr_b16 v[28:29], v7 offset:8192\n"
  "v_mfma_f32_16x16x32_bf16 v[136:139], v[32:35], v[48:51], v[136:139]\n"
  "ds_read_b64_tr_b16 v[30:31], v7 offset:10240\n"
  "s_waitcnt lgkmcnt(8)\n"
  "v_mfma_f32_16x16x32_bf16 v[144:147], v[36:39], v[48:51], v[144:147]\n"
  "ds_read_b64_tr_b16 v[32:33], v8 offset:8192\n"
  "v_mfma_f32_16x16x32_bf16 v[148:151], v[36:39], v[52:55], v[148:151]\n"
  "ds_read_b64_tr_b16 v[34:35], v8 offset:10240\n"
  "s_waitcnt lgkmcnt(8)\n"
  "v_mfma_f32_16x16x32_bf16 v[156:159], v[40:43], v[52:55], v[156:159]\n"
  "ds_read_b64_tr_b16 v[36:37], v9 offset:8192\n"
  "v_mfma_f32_16x16x32_bf16 v[152:155], v[40:43], v[48:51], v[152:155]\n"
  "ds_read_b64_tr_b16 v[38:39], v9 offset:10240\n"
  "s_waitcnt lgkmcnt(8)\n"
  "v_mfma_f32_16x16x32_bf16 v[160:163], v[44:47], v[48:51], v[160:163]\n"
  "ds_read_b64_tr_b16 v[40:41], v6 offset:4096\n"
  "v_mfma_f32_16x16x32_bf16 v[164:167], v[44:47], v[52:55], v[164:167]\n"
  "ds_read_b64_tr_b16 v[42:43], v6 offset:6144\n"
  "s_waitcnt lgkmcnt(6)\n"
  "v_mfma_f32_16x16x32_bf16 v[172:175], v[28:31], v[52:55], v[172:175]\n"
  "ds_read_b64_tr_b16 v[44:45], v7 offset:4096\n"
  "v_mfma_f32_16x16x32_bf16 v[168:171], v[28:31], v[48:51], v[168:171]\n"
  "ds_read_b64_tr_b16 v[46:47], v7 offset:6144\n"
  "s_waitcnt lgkmcnt(6)\n"
  "v_mfma_f32_16x16x32_bf16 v[176:179], v[32:35], v[48:51], v[176:179]\n"
  "ds_read_b64_tr_b16 v[28:29], v8 offset:4096\n"
  "v_mfma_f32_16x16x32_bf16 v[180:183], v[32:35], v[52:55], v[180:183]\n"
  "ds_read_b64_tr_b16 v[30:31], v8 offset:6144\n"
  "s_waitcnt lgkmcnt(6)\n"
  "v_mfma_f32_16x16x32_bf16 v[188:191], v[36:39], v[52:55], v[188:191]\n"
  "ds_read_b64_tr_b16 v[32:33], v9 offset:4096\n"
  "v_mfma_f32_16x16x32_bf16 v[184:187], v[36:39], v[48:51], v[184:187]\n"
  "ds_read_b64_tr_b16 v[34:35], v9 offset:6144\n"
  "s_waitcnt lgkmcnt(6)\n"
  "v_mfma_f32_16x16x32_bf16 v[128:131], v[40:43], v[56:59], v[128:131]\n"
  "ds_read_b64_tr_b16 v[36:37], v6 offset:12288\n"
  "v_mfma_f32_16x16x32_bf16 v[132:135], v[40:43], v[60:63], v[132:135]\n"
  "ds_read_b64_tr_b16 v[38:39], v6 offset:14336\n"
  "s_waitcnt lgkmcnt(6)\n"
  "v_mfma_f32_16x16x32_bf16 v[140:143], v[44:47], v[60:63], v[140:143]\n"
  "ds_read_b64_tr_b16 v[40:41], v7 offset:12288\n"
  "v_mfma_f32_16x16x32_bf16 v[136:139], v[44:47], v[56:59], v[136:139]\n"
  "ds_read_b64_tr_b16 v[42:43], v7 offset:14336\n"
  "s_waitcnt lgkmcnt(6)\n"
  "v_mfma_f32_16x16x32_bf16 v[144:147], v[28:31], v[56:59], v[144:147]\n"
  "ds_read_b64_tr_b16 v[44:45], v8 offset:12288\n"
  "v_mfma_f32_16x16x32_bf16 v[148:151], v[28:31], v[60:63], v[148:151]\n"
  "ds_read_b64_tr_b16 v[46:47], v8 offset:14336\n"
  "s_waitcnt lgkmcnt(6)\n"
  "v_mfma_f32_16x16x32_bf16 v[156:159], v[32:35], v[60:63], v[156:159]\n"
  "ds_read_b64_tr_b16 v[28:29], v9 offset:12288\n"
  "v_mfma_f32_16x16x32_bf16 v[152:155], v[32:35], v[56:59], v[152:155]\n"
  "ds_read_b64_tr_b16 v[30:31], v9 offset:14336\n"
  "s_waitcnt lgkmcnt(6)\n"
  "v_mfma_f32_16x16x32_bf16 v[160:163], v[36:39], v[56:59], v[160:163]\n"
  "s_barrier\n"
  "ds_read_b64_tr_b16 v[32:33], v6 offset:16384\n"
  "v_mfma_f32_16x16x32_bf16 v[164:167], v[36:39], v[60:63], v[164:167]\n"
  "ds_read_b64_tr_b16 v[34:35], v6 offset:18432\n"
  "ds_read_b128 v[48:51], v12 offset:49152\n"
  "ds_read_b128 v[52:55], v12 offset:50176\n"
  "s_waitcnt lgkmcnt(8)\n"
  "v_mfma_f32_16x16x32_bf16 v[172:175], v[40:43], v[60:63], v[172:175]\n"
  "ds_read_b64_tr_b16 v[36:37], v7 offset:16384\n"
  "v_mfma_f32_16x16x32_bf16 v[168:171], v[40:43], v[56:59], v[168:171]\n"
  "ds_read_b64_tr_b16 v[38:39], v7 offset:18432\n"
  "s_waitcnt lgkmcnt(8)\n"
  "v_mfma_f32_16x16x32_bf16 v[176:179], v[44:47], v[56:59], v[176:179]\n"
  "ds_read_b64_tr_b16 v[40:41], v8 offset:16384\n"
  "v_mfma_f32_16x16x32_bf16 v[180:183], v[44:47], v[60:63], v[180:183]\n"
  "ds_read_b64_tr_b16 v[42:43], v8 offset:18432\n"
  "s_waitcnt lgkmcnt(8)\n"
  "v_mfma_f32_16x16x32_bf16 v[188:191], v[28:31], v[60:63], v[188:191]\n"
  "ds_read_b64_tr_b16 v[44:45], v9 offset:16384\n"
  "v_mfma_f32_16x16x32_bf16 v[184:187], v[28:31], v[56:59], v[184:187]\n"
  "ds_read_b64_tr_b16 v[46:47], v9 offset:18432\n"
  "s_waitcnt lgkmcnt(7)\n"
  "v_mfma_f32_16x16x32_bf16 v[128:131], v[32:35], v[48:51], v[128:131]\n"
  "ds_read_b64_tr_b16 v[28:29], v6 offset:24576\n"
  "s_waitcnt lgkmcnt(7)\n"
  "v_mfma_f32_16x16x32_bf16 v[132:135], v[32:35], v[52:55], v[132:135]\n"
  "ds_read_b64_tr_b16 v[30:31], v6 offset:26624\n"
  "ds_read_b128 v[56:59], v12 offset:51200\n"
  "ds_read_b128 v[60:63], v12 offset:52224\n"
  "s_waitcnt lgkmcnt(8)\n"
  "v_mfma_f32_16x16x32_bf16 v[140:143], v[36:39], v[52:55], v[140:143]\n"
  "ds_read_b64_tr_b16 v[32:33], v7 offset:24576\n"
  "v_mfma_f32_16x16x32_bf16 v[136:139], v[36:39], v[48:51], v[136:139]\n"
  "ds_read_b64_tr_b16 v[34:35], v7 offset:26624\n"
  "s_waitcnt lgkmcnt(8)\n"
  "v_mfma_f32_16x16x32_bf16 v[144:147], v[40:43], v[48:51], v[144:147]\n"
  "ds_read_b64_tr_b16 v[36:37], v8 offset:24576\n"
  "v_mfma_f32_16x16x32_bf16 v[148:151], v[40:43], v[52:55], v[148:151]\n"
  "ds_read_b64_tr_b16 v[38:39], v8 offset:26624\n"
  "s_waitcnt lgkmcnt(8)\n"
  "v_mfma_f32_16x16x32_bf16 v[156:159], v[44:47], v[52:55], v[156:159]\n"
  "ds_read_b64_tr_b16 v[40:41], v9 offset:24576\n"
  "v_mfma_f32_16x16x32_bf16 v[152:155], v[44:47], v[48:51], v[152:155]\n"
  "ds_read_b64_tr_b16 v[42:43], v9 offset:26624\n"
  "s_waitcnt lgkmcnt(8)\n"
  "v_mfma_f32_16x16x32_bf16 v[160:163], v[28:31], v[48:51], v[160:163]\n"
  "ds_read_b64_tr_b16 v[44:45], v6 offset:20480\n"
  "v_mfma_f32_16x16x32_bf16 v[164:167], v[28:31], v[52:55], v[164:167]\n"
  "ds_read_b64_tr_b16 v[46:47], v6 offset:22528\n"
  "s_waitcnt lgkmcnt(6)\n"
  "v_mfma_f32_16x16x32_bf16 v[172:175], v[32:35], v[52:55], v[172:175]\n"
  "ds_read_b64_tr_b16 v[28:29], v7 offset:20480\n"
  "v_mfma_f32_16x16x32_bf16 v[168:171], v[32:35], v[48:51], v[168:171]\n"
  "ds_read_b64_tr_b16 v[30:31], v7 offset:22528\n"
  "s_waitcnt lgkmcnt(6)\n"
  "v_mfma_f32_16x16x32_bf16 v[176:179], v[36:39], v[48:51], v[176:179]\n"
  "ds_read_b64_tr_b16 v[32:33], v8 offset:20480\n"
  "v_mfma_f32_16x16x32_bf16 v[180:183], v[36:39], v[52:55], v[180:183]\n"
  "ds_read_b64_tr_b16 v[34:35], v8 offset:22528\n"
  "s_waitcnt lgkmcnt(6)\n"
  "v_mfma_f32_16x16x32_bf16 v[188:191], v[40:43], v[52:55], v[188:191]\n"
  "ds_read_b64_tr_b16 v[36:37], v9 offset:20480\n"
  "v_mfma_f32_16x16x32_bf16 v[184:187], v[40:43], v[48:51], v[184:187]\n"
  "ds_read_b64_tr_b16 v[38:39], v9 offset:22528\n"
  "s_waitcnt lgkmcnt(6)\n"
  "v_mfma_f32_16x16x32_bf16 v[128:131], v[44:47], v[56:59], v[128:131]\n"
  "ds_read_b64_tr_b16 v[40:41], v6 offset:28672\n"
  "v_mfma_f32_16x16x32_bf16 v[132:135], v[44:47], v[60:63], v[132:135]\n"
  "ds_read_b64_tr_b16 v[42:43], v6 offset:30720\n"
  "s_waitcnt lgkmcnt(6)\n"
  "v_mfma_f32_16x16x32_bf16 v[140:143], v[28:31], v[60:63], v[140:143]\n"
  "ds_read_b64_tr_b16 v[44:45], v7 offset:28672\n"
  "v_mfma_f32_16x16x32_bf16 v[136:139], v[28:31], v[56:59], v[136:139]\n"
  "ds_read_b64_tr_b16 v[46:47], v7 offset:30720\n"
  "s_waitcnt lgkmcnt(6)\n"
  "v_mfma_f32_16x16x32_bf16 v[144:147], v[32:35], v[56:59], v[144:147]\n"
  "ds_read_b64_tr_b16 v[28:29], v8 offset:28672\n"
  "v_mfma_f32_16x16x32_bf16 v[148:151], v[32:35], v[60:63], v[148:151]\n"
  "ds_read_b64_tr_b16 v[30:31], v8 offset:30720\n"
  "s_waitcnt lgkmcnt(6)\n"
  "v_mfma_f32_16x16x32_bf16 v[156:159], v[36:39], v[60:63], v[156:159]\n"
  "ds_read_b64_tr_b16 v[32:33], v9 offset:28672\n"
  "v_mfma_f32_16x16x32_bf16 v[152:155], v[36:39], v[56:59], v[152:155]\n"
  "ds_read_b64_tr_b16 v[34:35], v9 offset:30720\n"
  "s_waitcnt lgkmcnt(6)\n"
  "v_mfma_f32_16x16x32_bf16 v[160:163], v[40:43], v[56:59], v[160:163]\n"
  "s_barrier\n"
  "v_add_u32 v6, 131072, v6\n"
  "v_add_u32 v7, 131072, v7\n"
  "v_add_u32 v8, 131072, v8\n"
  "v_add_u32 v9, 131072, v9\n"
  "ds_read_b64_tr_b16 v[36:37], v6 offset:0\n"
  "v_mfma_f32_16x16x32_bf16 v[164:167], v[40:43], v[60:63], v[164:167]\n"
  "ds_read_b64_tr_b16 v[38:39], v6 offset:2048\n"
  "ds_read_b128 v[64:67], v12 offset:0\n"
  "ds_read_b128 v[68:71], v12 offset:1024\n"
  "s_waitcnt lgkmcnt(8)\n"
  "v_mfma_f32_16x16x32_bf16 v[172:175], v[44:47], v[60:63], v[172:175]\n"
  "ds_read_b64_tr_b16 v[40:41], v7 offset:0\n"
  "v_mfma_f32_16x16x32_bf16 v[168:171], v[44:47], v[56:59], v[168:171]\n"
  "ds_read_b64_tr_b16 v[42:43], v7 offset:2048\n"
  "s_waitcnt lgkmcnt(8)\n"
  "v_mfma_f32_16x16x32_bf16 v[176:179], v[28:31], v[56:59], v[176:179]\n"
  "ds_read_b64_tr_b16 v[44:45], v8 offset:0\n"
  "v_mfma_f32_16x16x32_bf16 v[180:183], v[28:31], v[60:63], v[180:183]\n"
  "ds_read_b64_tr_b16 v[46:47], v8 offset:2048\n"
  "ds_read_b128 v[72:75], v12 offset:2048\n"
  "ds_read_b128 v[76:79], v12 offset:3072\n"
  "s_waitcnt lgkmcnt(10)\n"
  "v_mfma_f32_16x16x32_bf16 v[188:191], v[32:35], v[60:63], v[188:191]\n"
  "ds_read_b64_tr_b16 v[28:29], v9 offset:0\n"
  "v_mfma_f32_16x16x32_bf16 v[184:187], v[32:35], v[56:59], v[184:187]\n"
  "ds_read_b64_tr_b16 v[30:31], v9 offset:2048\n"
  "s_waitcnt lgkmcnt(9)\n"
  "v_mfma_f32_16x16x32_bf16 v[128:131], v[36:39], v[64:67], v[128:131]\n"
  "ds_read_b64_tr_b16 v[32:33], v6 offset:8192\n"
  "s_waitcnt lgkmcnt(9)\n"
  "v_mfma_f32_16x16x32_bf16 v[132:135], v[36:39], v[68:71], v[132:135]\n"
  "ds_read_b64_tr_b16 v[34:35], v6 offset:10240\n"
  "s_waitcnt lgkmcnt(8)\n"
  "v_mfma_f32_16x16x32_bf16 v[140:143], v[40:43], v[68:71], v[140:143]\n"
  "ds_read_b64_tr_b16 v[36:37], v7 offset:8192\n"
  "v_mfma_f32_16x16x32_bf16 v[136:139], v[40:43], v[64:67], v[136:139]\n"
  "ds_read_b64_tr_b16 v[38:39], v7 offset:10240\n"
  "s_waitcnt lgkmcnt(8)\n"
  "v_mfma_f32_16x16x32_bf16 v[144:147], v[44:47], v[64:67], v[144:147]\n"
  "ds_read_b64_tr_b16 v[40:41], v6 offset:4096\n"
  "v_mfma_f32_16x16x32_bf16 v[148:151], v[44:47], v[68:71], v[148:151]\n"
  "ds_read_b64_tr_b16 v[42:43], v6 offset:6144\n"
  "s_waitcnt lgkmcnt(6)\n"
  "v_mfma_f32_16x16x32_bf16 v[156:159], v[28:31], v[68:71], v[156:159]\n"
  "ds_read_b64_tr_b16 v[44:45], v7 offset:4096\n"
  "v_mfma_f32_16x16x32_bf16 v[152:155], v[28:31], v[64:67], v[152:155]\n"
  "ds_read_b64_tr_b16 v[46:47], v7 offset:6144\n"
  "s_waitcnt lgkmcnt(6)\n"
  "v_mfma_f32_16x16x32_bf16 v[160:163], v[32:35], v[64:67], v[160:163]\n"
  "ds_read_b64_tr_b16 v[28:29], v8 offset:4096\n"
  "v_mfma_f32_16x16x32_bf16 v[164:167], v[32:35], v[68:71], v[164:167]\n"
  "ds_read_b64_tr_b16 v[30:31], v8 offset:6144\n"
  "s_waitcnt lgkmcnt(6)\n"
  "v_mfma_f32_16x16x32_bf16 v[172:175], v[36:39], v[68:71], v[172:175]\n"
  "ds_read_b64_tr_b16 v[32:33], v9 offset:4096\n"
  "v_mfma_f32_16x16x32_bf16 v[168:171], v[36:39], v[64:67], v[168:171]\n"
  "ds_read_b64_tr_b16 v[34:35], v9 offset:6144\n"
  "s_waitcnt lgkmcnt(6)\n"
  "v_mfma_f32_16x16x32_bf16 v[128:131], v[40:43], v[72:75], v[128:131]\n"
  "ds_read_b64_tr_b16 v[36:37], v6 offset:12288\n"
  "v_mfma_f32_16x16x32_bf16 v[132:135], v[40:43], v[76:79], v[132:135]\n"
  "ds_read_b64_tr_b16 v[38:39], v6 offset:14336\n"
  "s_waitcnt lgkmcnt(6)\n"
  "v_mfma_f32_16x16x32_bf16 v[140:143], v[44:47], v[76:79], v[140:143]\n"
  "ds_read_b64_tr_b16 v[40:41], v7 offset:12288\n"
  "v_mfma_f32_16x16x32_bf16 v[136:139], v[44:47], v[72:75], v[136:139]\n"
  "ds_read_b64_tr_b16 v[42:43], v7 offset:14336\n"
  "s_waitcnt lgkmcnt(6)\n"
  "v_mfma_f32_16x16x32_bf16 v[144:147], v[28:31], v[72:75], v[144:147]\n"
  "s_barrier\n"
  "ds_read_b64_tr_b16 v[44:45], v6 offset:16384\n"
  "v_mfma_f32_16x16x32_bf16 v[148:151], v[28:31], v[76:79], v[148:151]\n"
  "ds_read_b64_tr_b16 v[46:47], v6 offset:18432\n"
  "ds_read_b128 v[80:83], v12 offset:16384\n"
  "ds_read_b128 v[84:87], v12 offset:17408\n"
  "s_waitcnt lgkmcnt(8)\n"
  "v_mfma_f32_16x16x32_bf16 v[156:159], v[32:35], v[76:79], v[156:159]\n"
  "ds_read_b64_tr_b16 v[28:29], v7 offset:16384\n"
  "v_mfma_f32_16x16x32_bf16 v[152:155], v[32:35], v[72:75], v[152:155]\n"
  "ds_read_b64_tr_b16 v[30:31], v7 offset:18432\n"
  "s_waitcnt lgkmcnt(8)\n"
  "v_mfma_f32_16x16x32_bf16 v[160:163], v[36:39], v[72:75], v[160:163]\n"
  "ds_read_b64_tr_b16 v[32:33], v8 offset:16384\n"
  "v_mfma_f32_16x16x32_bf16 v[164:167], v[36:39], v[76:79], v[164:167]\n"
  "ds_read_b64_tr_b16 v[34:35], v8 offset:18432\n"
  "ds_read_b128 v[88:91], v12 offset:18432\n"
  "ds_read_b128 v[92:95], v12 offset:19456\n"
  "s_waitcnt lgkmcnt(10)\n"
  "v_mfma_f32_16x16x32_bf16 v[172:175], v[40:43], v[76:79], v[172:175]\n"
  "ds_read_b64_tr_b16 v[36:37], v9 offset:16384\n"
  "v_mfma_f32_16x16x32_bf16 v[168:171], v[40:43], v[72:75], v[168:171]\n"
  "ds_read_b64_tr_b16 v[38:39], v9 offset:18432\n"
  "s_waitcnt lgkmcnt(9)\n"
  "v_mfma_f32_16x16x32_bf16 v[128:131], v[44:47], v[80:83], v[128:131]\n"
  "ds_read_b64_tr_b16 v[40:41], v6 offset:24576\n"
  "s_waitcnt lgkmcnt(9)\n"
  "v_mfma_f32_16x16x32_bf16 v[132:135], v[44:47], v[84:87], v[132:135]\n"
  "ds_read_b64_tr_b16 v[42:43], v6 offset:26624\n"
  "s_waitcnt lgkmcnt(8)\n"
  "v_mfma_f32_16x16x32_bf16 v[140:143], v[28:31], v[84:87], v[140:143]\n"
  "ds_read_b64_tr_b16 v[44:45], v7 offset:24576\n"
  "v_mfma_f32_16x16x32_bf16 v[136:139], v[28:31], v[80:83], v[136:139]\n"
  "ds_read_b64_tr_b16 v[46:47], v7 offset:26624\n"
  "s_waitcnt lgkmcnt(8)\n"
  "v_mfma_f32_16x16x32_bf16 v[144:147], v[32:35], v[80:83], v[144:147]\n"
  "ds_read_b64_tr_b16 v[28:29], v6 offset:20480\n"
  "v_mfma_f32_16x16x32_bf16 v[148:151], v[32:35], v[84:87], v[148:151]\n"
  "ds_read_b64_tr_b16 v[30:31], v6 offset:22528\n"
  "s_waitcnt lgkmcnt(6)\n"
  "v_mfma_f32_16x16x32_bf16 v[156:159], v[36:39], v[84:87], v[156:159]\n"
  "ds_read_b64_tr_b16 v[32:33], v7 offset:20480\n"
  "v_mfma_f32_16x16x32_bf16 v[152:155], v[36:39], v[80:83], v[152:155]\n"
  "ds_read_b64_tr_b16 v[34:35], v7 offset:22528\n"
  "s_waitcnt lgkmcnt(6)\n"
  "v_mfma_f32_16x16x32_bf16 v[160:163], v[40:43], v[80:83], v[160:163]\n"
  "ds_read_b64_tr_b16 v[36:37], v8 offset:20480\n"
  "v_mfma_f32_16x16x32_bf16 v[164:167], v[40:43], v[84:87], v[164:167]\n"
  "ds_read_b64_tr_b16 v[38:39], v8 offset:22528\n"
  "s_waitcnt lgkmcnt(6)\n"
  "v_mfma_f32_16x16x32_bf16 v[172:175], v[44:47], v[84:87], v[172:175]\n"
  "ds_read_b64_tr_b16 v[40:41], v9 offset:20480\n"
  "v_mfma_f32_16x16x32_bf16 v[168:171], v[44:47], v[80:83], v[168:171]\n"
  "ds_read_b64_tr_b16 v[42:43], v9 offset:22528\n"
  "s_waitcnt lgkmcnt(6)\n"
  "v_mfma_f32_16x16x32_bf16 v[128:131], v[28:31], v[88:91], v[128:131]\n"
  "ds_read_b64_tr_b16 v[44:45], v6 offset:28672\n"
  "v_mfma_f32_16x16x32_bf16 v[132:135], v[28:31], v[92:95], v[132:135]\n"
  "ds_read_b64_tr_b16 v[46:47], v6 offset:30720\n"
  "s_waitcnt lgkmcnt(6)\n"
  "v_mfma_f32_16x16x32_bf16 v[140:143], v[32:35], v[92:95], v[140:143]\n"
  "ds_read_b64_tr_b16 v[28:29], v7 offset:28672\n"
  "v_mfma_f32_16x16x32_bf16 v[136:139], v[32:35], v[88:91], v[136:139]\n"
  "ds_read_b64_tr_b16 v[30:31], v7 offset:30720\n"
  "s_waitcnt lgkmcnt(6)\n"
  "v_mfma_f32_16x16x32_bf16 v[144:147], v[36:39], v[88:91], v[144:147]\n"
  "s_barrier\n"
  "v_add_u32 v6, 4294836224, v6\n"
  "v_add_u32 v7, 4294836224, v7\n"
  "v_add_u32 v8, 4294836224, v8\n"
  "v_add_u32 v9, 4294836224, v9\n"
  "ds_read_b64_tr_b16 v[32:33], v6 offset:32768\n"
  "v_mfma_f32_16x16x32_bf16 v[148:151], v[36:39], v[92:95], v[148:151]\n"
  "ds_read_b64_tr_b16 v[34:35], v6 offset:34816\n"
  "ds_read_b128 v[96:99], v12 offset:32768\n"
  "ds_read_b128 v[100:103], v12 offset:33792\n"
  "s_waitcnt lgkmcnt(8)\n"
  "v_mfma_f32_16x16x32_bf16 v[156:159], v[40:43], v[92:95], v[156:159]\n"
  "ds_read_b64_tr_b16 v[36:37], v7 offset:32768\n"
  "v_mfma_f32_16x16x32_bf16 v[152:155], v[40:43], v[88:91], v[152:155]\n"
  "ds_read_b64_tr_b16 v[38:39], v7 offset:34816\n"
  "s_waitcnt lgkmcnt(8)\n"
  "v_mfma_f32_16x16x32_bf16 v[160:163], v[44:47], v[88:91], v[160:163]\n"
  "ds_read_b64_tr_b16 v[40:41], v8 offset:32768\n"
  "v_mfma_f32_16x16x32_bf16 v[164:167], v[44:47], v[92:95], v[164:167]\n"
  "ds_read_b64_tr_b16 v[42:43], v8 offset:34816\n"
  "ds_read_b128 v[104:107], v12 offset:34816\n"
  "ds_read_b128 v[108:111], v12 offset:35840\n"
  "s_waitcnt lgkmcnt(10)\n"
  "v_mfma_f32_16x16x32_bf16 v[172:175], v[28:31], v[92:95], v[172:175]\n"
  "ds_read_b64_tr_b16 v[44:45], v9 offset:32768\n"
  "v_mfma_f32_16x16x32_bf16 v[168:171], v[28:31], v[88:91], v[168:171]\n"
  "ds_read_b64_tr_b16 v[46:47], v9 offset:34816\n"
  "s_waitcnt lgkmcnt(9)\n"
  "v_mfma_f32_16x16x32_bf16 v[128:131], v[32:35], v[96:99], v[128:131]\n"
  "ds_read_b64_tr_b16 v[28:29], v6 offset:40960\n"
  "s_waitcnt lgkmcnt(9)\n"
  "v_mfma_f32_16x16x32_bf16 v[132:135], v[32:35], v[100:103], v[132:135]\n"
  "ds_read_b64_tr_b16 v[30:31], v6 offset:43008\n"
  "s_waitcnt lgkmcnt(8)\n"
  "v_mfma_f32_16x16x32_bf16 v[140:143], v[36:39], v[100:103], v[140:143]\n"
  "ds_read_b64_tr_b16 v[32:33], v7 offset:40960\n"
  "v_mfma_f32_16x16x32_bf16 v[136:139], v[36:39], v[96:99], v[136:139]\n"
  "ds_read_b64_tr_b16 v[34:35], v7 offset:43008\n"
  "s_waitcnt lgkmcnt(8)\n"
  "v_mfma_f32_16x16x32_bf16 v[144:147], v[40:43], v[96:99], v[144:147]\n"
  "ds_read_b64_tr_b16 v[36:37], v6 offset:36864\n"
  "v_mfma_f32_16x16x32_bf16 v[148:151], v[40:43], v[100:103], v[148:151]\n"
  "ds_read_b64_tr_b16 v[38:39], v6 offset:38912\n"
  "s_waitcnt lgkmcnt(6)\n"
  "v_mfma_f32_16x16x32_bf16 v[156:159], v[44:47], v[100:103], v[156:159]\n"
  "ds_read_b64_tr_b16 v[40:41], v7 offset:36864\n"
  "v_mfma_f32_16x16x32_bf16 v[152:155], v[44:47], v[96:99], v[152:155]\n"
  "ds_read_b64_tr_b16 v[42:43], v7 offset:38912\n"
  "s_waitcnt lgkmcnt(6)\n"
  "v_mfma_f32_16x16x32_bf16 v[160:163], v[28:31], v[96:99], v[160:163]\n"
  "ds_read_b64_tr_b16 v[44:45], v8 offset:36864\n"
  "v_mfma_f32_16x16x32_bf16 v[164:167], v[28:31], v[100:103], v[164:167]\n"
  "ds_read_b64_tr_b16 v[46:47], v8 offset:38912\n"
  "s_waitcnt lgkmcnt(6)\n"
  "v_mfma_f32_16x16x32_bf16 v[172:175], v[32:35], v[100:103], v[172:175]\n"
  "ds_read_b64_tr_b16 v[28:29], v9 offset:36864\n"
  "v_mfma_f32_16x16x32_bf16 v[168:171], v[32:35], v[96:99], v[168:171]\n"
  "ds_read_b64_tr_b16 v[30:31], v9 offset:38912\n"
  "s_waitcnt lgkmcnt(6)\n"
  "v_mfma_f32_16x16x32_bf16 v[128:131], v[36:39], v[104:107], v[128:131]\n"
  "ds_read_b64_tr_b16 v[32:33], v6 offset:45056\n"
  "v_mfma_f32_16x16x32_bf16 v[132:135], v[36:39], v[108:111], v[132:135]\n"
  "ds_read_b64_tr_b16 v[34:35], v6 offset:47104\n"
  "s_waitcnt lgkmcnt(6)\n"
  "v_mfma_f32_16x16x32_bf16 v[140:143], v[40:43], v[108:111], v[140:143]\n"
  "ds_read_b64_tr_b16 v[36:37], v7 offset:45056\n"
  "v_mfma_f32_16x16x32_bf16 v[136:139], v[40:43], v[104:107], v[136:139]\n"
  "ds_read_b64_tr_b16 v[38:39], v7 offset:47104\n"
  "s_waitcnt lgkmcnt(6)\n"
  "v_mfma_f32_16x16x32_bf16 v[144:147], v[44:47], v[104:107], v[144:147]\n"
  "s_barrier\n"
  "ds_read_b64_tr_b16 v[40:41], v6 offset:49152\n"
  "v_mfma_f32_16x16x32_bf16 v[148:151], v[44:47], v[108:111], v[148:151]\n"
  "ds_read_b64_tr_b16 v[42:43], v6 offset:51200\n"
  "ds_read_b128 v[112:115], v12 offset:49152\n"
  "ds_read_b128 v[116:119], v12 offset:50176\n"
  "s_waitcnt lgkmcnt(8)\n"
  "v_mfma_f32_16x16x32_bf16 v[156:159], v[28:31], v[108:111], v[156:159]\n"
  "ds_read_b64_tr_b16 v[44:45], v7 offset:49152\n"
  "v_mfma_f32_16x16x32_bf16 v[152:155], v[28:31], v[104:107], v[152:155]\n"
  "ds_read_b64_tr_b16 v[46:47], v7 offset:51200\n"
  "s_waitcnt lgkmcnt(8)\n"
  "v_mfma_f32_16x16x32_bf16 v[160:163], v[32:35], v[104:107], v[160:163]\n"
  "ds_read_b64_tr_b16 v[28:29], v8 offset:49152\n"
  "v_mfma_f32_16x16x32_bf16 v[164:167], v[32:35], v[108:111], v[164:167]\n"
  "ds_read_b64_tr_b16 v[30:31], v8 offset:51200\n"
  "ds_read_b128 v[120:123], v12 offset:51200\n"
  "ds_read_b128 v[124:127], v12 offset:52224\n"
  "s_waitcnt lgkmcnt(10)\n"
  "v_mfma_f32_16x16x32_bf16 v[172:175], v[36:39], v[108:111], v[172:175]\n"
  "ds_read_b64_tr_b16 v[32:33], v9 offset:49152\n"
  "v_mfma_f32_16x16x32_bf16 v[168:171], v[36:39], v[104:107], v[168:171]\n"
  "ds_read_b64_tr_b16 v[34:35], v9 offset:51200\n"
  "s_waitcnt lgkmcnt(9)\n"
  "v_mfma_f32_16x16x32_bf16 v[128:131], v[40:43], v[112:115], v[128:131]\n"
  "ds_read_b64_tr_b16 v[36:37], v6 offset:57344\n"
  "s_waitcnt lgkmcnt(9)\n"
  "v_mfma_f32_16x16x32_bf16 v[132:135], v[40:43], v[116:119], v[132:135]\n"
  "ds_read_b64_tr_b16 v[38:39], v6 offset:59392\n"
  "s_waitcnt lgkmcnt(8)\n"
  "v_mfma_f32_16x16x32_bf16 v[140:143], v[44:47], v[116:119], v[140:143]\n"
  "ds_read_b64_tr_b16 v[40:41], v7 offset:57344\n"
  "v_mfma_f32_16x16x32_bf16 v[136:139], v[44:47], v[112:115], v[136:139]\n"
  "ds_read_b64_tr_b16 v[42:43], v7 offset:59392\n"
  "s_waitcnt lgkmcnt(8)\n"
  "v_mfma_f32_16x16x32_bf16 v[144:147], v[28:31], v[112:115], v[144:147]\n"
  "ds_read_b64_tr_b16 v[44:45], v6 offset:53248\n"
  "v_mfma_f32_16x16x32_bf16 v[148:151], v[28:31], v[116:119], v[148:151]\n"
  "ds_read_b64_tr_b16 v[46:47], v6 offset:55296\n"
  "s_waitcnt lgkmcnt(6)\n"
  "v_mfma_f32_16x16x32_bf16 v[156:159], v[32:35], v[116:119], v[156:159]\n"
  "ds_read_b64_tr_b16 v[28:29], v7 offset:53248\n"
  "v_mfma_f32_16x16x32_bf16 v[152:155], v[32:35], v[112:115], v[152:155]\n"
  "ds_read_b64_tr_b16 v[30:31], v7 offset:55296\n"
  "s_waitcnt lgkmcnt(6)\n"
  "v_mfma_f32_16x16x32_bf16 v[160:163], v[36:39], v[112:115], v[160:163]\n"
  "ds_read_b64_tr_b16 v[32:33], v8 offset:53248\n"
  "v_mfma_f32_16x16x32_bf16 v[164:167], v[36:39], v[116:119], v[164:167]\n"
  "ds_read_b64_tr_b16 v[34:35], v8 offset:55296\n"
  "s_waitcnt lgkmcnt(6)\n"
  "v_mfma_f32_16x16x32_bf16 v[172:175], v[40:43], v[116:119], v[172:175]\n"
  "ds_read_b64_tr_b16 v[36:37], v9 offset:53248\n"
  "v_mfma_f32_16x16x32_bf16 v[168:171], v[40:43], v[112:115], v[168:171]\n"
  "ds_read_b64_tr_b16 v[38:39], v9 offset:55296\n"
  "s_waitcnt lgkmcnt(6)\n"
  "v_mfma_f32_16x16x32_bf16 v[128:131], v[44:47], v[120:123], v[128:131]\n"
  "ds_read_b64_tr_b16 v[40:41], v6 offset:61440\n"
  "v_mfma_f32_16x16x32_bf16 v[132:135], v[44:47], v[124:127], v[132:135]\n"
  "ds_read_b64_tr_b16 v[42:43], v6 offset:63488\n"
  "s_waitcnt lgkmcnt(6)\n"
  "v_mfma_f32_16x16x32_bf16 v[140:143], v[28:31], v[124:127], v[140:143]\n"
  "ds_read_b64_tr_b16 v[44:45], v7 offset:61440\n"
  "v_mfma_f32_16x16x32_bf16 v[136:139], v[28:31], v[120:123], v[136:139]\n"
  "ds_read_b64_tr_b16 v[46:47], v7 offset:63488\n"
  "s_waitcnt lgkmcnt(6)\n"
  "v_mfma_f32_16x16x32_bf16 v[144:147], v[32:35], v[120:123], v[144:147]\n"
  "v_add_u32 v6, 131072, v6\n"
  "v_add_u32 v7, 131072, v7\n"
  "v_add_u32 v8, 131072, v8\n"
  "v_add_u32 v9, 131072, v9\n"
  "ds_read_b64_tr_b16 v[28:29], v8 offset:8192\n"
  "v_mfma_f32_16x16x32_bf16 v[148:151], v[32:35], v[124:127], v[148:151]\n"
  "ds_read_b64_tr_b16 v[30:31], v8 offset:10240\n"
  "s_waitcnt lgkmcnt(6)\n"
  "v_mfma_f32_16x16x32_bf16 v[156:159], v[36:39], v[124:127], v[156:159]\n"
  "ds_read_b64_tr_b16 v[32:33], v9 offset:8192\n"
  "v_mfma_f32_16x16x32_bf16 v[152:155], v[36:39], v[120:123], v[152:155]\n"
  "ds_read_b64_tr_b16 v[34:35], v9 offset:10240\n"
  "s_waitcnt lgkmcnt(6)\n"
  "v_mfma_f32_16x16x32_bf16 v[160:163], v[40:43], v[120:123], v[160:163]\n"
  "ds_read_b64_tr_b16 v[36:37], v8 offset:12288\n"
  "v_mfma_f32_16x16x32_bf16 v[164:167], v[40:43], v[124:127], v[164:167]\n"
  "ds_read_b64_tr_b16 v[38:39], v8 offset:14336\n"
  "s_waitcnt lgkmcnt(6)\n"
  "v_mfma_f32_16x16x32_bf16 v[172:175], v[44:47], v[124:127], v[172:175]\n"
  "ds_read_b64_tr_b16 v[40:41], v9 offset:12288\n"
  "v_mfma_f32_16x16x32_bf16 v[168:171], v[44:47], v[120:123], v[168:171]\n"
  "ds_read_b64_tr_b16 v[42:43], v9 offset:14336\n"
  "s_waitcnt lgkmcnt(6)\n"
  "v_mfma_f32_16x16x32_bf16 v[176:179], v[28:31], v[64:67], v[176:179]\n"
  "ds_read_b64_tr_b16 v[44:45], v8 offset:24576\n"
  "v_mfma_f32_16x16x32_bf16 v[180:183], v[28:31], v[68:71], v[180:183]\n"
  "ds_read_b64_tr_b16 v[46:47], v8 offset:26624\n"
  "v_cvt_pk_bf16_f32 v192, v128, v129\n"
  "v_cvt_pk_bf16_f32 v193, v130, v131\n"
  "v_cvt_pk_bf16_f32 v194, v136, v137\n"
  "v_cvt_pk_bf16_f32 v195, v138, v139\n"
  "buffer_store_dwordx4 v[192:195], v1, s[24:27], s72 offen sc1\n"
  "v_cvt_pk_bf16_f32 v196, v132, v133\n"
  "v_cvt_pk_bf16_f32 v197, v134, v135\n"
  "v_cvt_pk_bf16_f32 v198, v140, v141\n"
  "v_cvt_pk_bf16_f32 v199, v142, v143\n"
  "buffer_store_dwordx4 v[196:199], v1, s[24:27], s72 offen offset:1024 sc1\n"
  "s_waitcnt lgkmcnt(6)\n"
  "v_mfma_f32_16x16x32_bf16 v[188:191], v[32:35], v[68:71], v[188:191]\n"
  "ds_read_b64_tr_b16 v[28:29], v9 offset:24576\n"
  "v_mfma_f32_16x16x32_bf16 v[184:187], v[32:35], v[64:67], v[184:187]\n"
  "ds_read_b64_tr_b16 v[30:31], v9 offset:26624\n"
  "v_cvt_pk_bf16_f32 v200, v144, v145\n"
  "v_cvt_pk_bf16_f32 v201, v146, v147\n"
  "v_cvt_pk_bf16_f32 v202, v152, v153\n"
  "v_cvt_pk_bf16_f32 v203, v154, v155\n"
  "buffer_store_dwordx4 v[200:203], v1, s[24:27], s73 offen sc1\n"
  "v_cvt_pk_bf16_f32 v204, v148, v149\n"
  "v_cvt_pk_bf16_f32 v205, v150, v151\n"
  "v_cvt_pk_bf16_f32 v206, v156, v157\n"
  "v_cvt_pk_bf16_f32 v207, v158, v159\n"
  "buffer_store_dwordx4 v[204:207], v1, s[24:27], s73 offen offset:1024 sc1\n"
  "s_waitcnt lgkmcnt(6)\n"
  "v_mfma_f32_16x16x32_bf16 v[176:179], v[36:39], v[72:75], v[176:179]\n"
  "ds_read_b64_tr_b16 v[32:33], v8 offset:28672\n"
  "v_mfma_f32_16x16x32_bf16 v[180:183], v[36:39], v[76:79], v[180:183]\n"
  "ds_read_b64_tr_b16 v[34:35], v8 offset:30720\n"
  "v_cvt_pk_bf16_f32 v208, v160, v161\n"
  "v_cvt_pk_bf16_f32 v209, v162, v163\n"
  "v_cvt_pk_bf16_f32 v210, v168, v169\n"
  "v_cvt_pk_bf16_f32 v211, v170, v171\n"
  "buffer_store_dwordx4 v[208:211], v1, s[24:27], s74 offen sc1\n"
  "v_cvt_pk_bf16_f32 v212, v164, v165\n"
  "v_cvt_pk_bf16_f32 v213, v166, v167\n"
  "v_cvt_pk_bf16_f32 v214, v172, v173\n"
  "v_cvt_pk_bf16_f32 v215, v174, v175\n"
  "buffer_store_dwordx4 v[212:215], v1, s[24:27], s74 offen offset:1024 sc1\n"
  "s_waitcnt lgkmcnt(6)\n"
  "v_mfma_f32_16x16x32_bf16 v[188:191], v[40:43], v[76:79], v[188:191]\n"
  "ds_read_b64_tr_b16 v[36:37], v9 offset:28672\n"
  "v_mfma_f32_16x16x32_bf16 v[184:187], v[40:43], v[72:75], v[184:187]\n"
  "ds_read_b64_tr_b16 v[38:39], v9 offset:30720\n"
  "s_waitcnt lgkmcnt(6)\n"
  "v_mfma_f32_16x16x32_bf16 v[176:179], v[44:47], v[80:83], v[176:179]\n"
  "v_add_u32 v6, 4294836224, v6\n"
  "v_add_u32 v7, 4294836224, v7\n"
  "v_add_u32 v8, 4294836224, v8\n"
  "v_add_u32 v9, 4294836224, v9\n"
  "ds_read_b64_tr_b16 v[40:41], v8 offset:40960\n"
  "v_mfma_f32_16x16x32_bf16 v[180:183], v[44:47], v[84:87], v[180:183]\n"
  "ds_read_b64_tr_b16 v[42:43], v8 offset:43008\n"
  "s_waitcnt lgkmcnt(6)\n"
  "v_mfma_f32_16x16x32_bf16 v[188:191], v[28:31], v[84:87], v[188:191]\n"
  "ds_read_b64_tr_b16 v[44:45], v9 offset:40960\n"
  "v_mfma_f32_16x16x32_bf16 v[184:187], v[28:31], v[80:83], v[184:187]\n"
  "ds_read_b64_tr_b16 v[46:47], v9 offset:43008\n"
  "s_waitcnt lgkmcnt(6)\n"
  "v_mfma_f32_16x16x32_bf16 v[176:179], v[32:35], v[88:91], v[176:179]\n"
  "ds_read_b64_tr_b16 v[28:29], v8 offset:45056\n"
  "v_mfma_f32_16x16x32_bf16 v[180:183], v[32:35], v[92:95], v[180:183]\n"
  "ds_read_b64_tr_b16 v[30:31], v8 offset:47104\n"
  "s_waitcnt lgkmcnt(6)\n"
  "v_mfma_f32_16x16x32_bf16 v[188:191], v[36:39], v[92:95], v[188:191]\n"
  "ds_read_b64_tr_b16 v[32:33], v9 offset:45056\n"
  "v_mfma_f32_16x16x32_bf16 v[184:187], v[36:39], v[88:91], v[184:187]\n"
  "ds_read_b64_tr_b16 v[34:35], v9 offset:47104\n"
  "s_waitcnt lgkmcnt(6)\n"
  "v_mfma_f32_16x16x32_bf16 v[176:179], v[40:43], v[96:99], v[176:179]\n"
  "ds_read_b64_tr_b16 v[36:37], v8 offset:57344\n"
  "v_mfma_f32_16x16x32_bf16 v[180:183], v[40:43], v[100:103], v[180:183]\n"
  "ds_read_b64_tr_b16 v[38:39], v8 offset:59392\n"
  "s_waitcnt lgkmcnt(6)\n"
  "v_mfma_f32_16x16x32_bf16 v[188:191], v[44:47], v[100:103], v[188:191]\n"
  "ds_read_b64_tr_b16 v[40:41], v9 offset:57344\n"
  "v_mfma_f32_16x16x32_bf16 v[184:187], v[44:47], v[96:99], v[184:187]\n"
  "ds_read_b64_tr_b16 v[42:43], v9 offset:59392\n"
  "s_waitcnt lgkmcnt(6)\n"
  "v_mfma_f32_16x16x32_bf16 v[176:179], v[28:31], v[104:107], v[176:179]\n"
  "ds_read_b64_tr_b16 v[44:45], v8 offset:61440\n"
  "v_mfma_f32_16x16x32_bf16 v[180:183], v[28:31], v[108:111], v[180:183]\n"
  "ds_read_b64_tr_b16 v[46:47], v8 offset:63488\n"
  "s_waitcnt lgkmcnt(6)\n"
  "v_mfma_f32_16x16x32_bf16 v[188:191], v[32:35], v[108:111], v[188:191]\n"
  "ds_read_b64_tr_b16 v[28:29], v9 offset:61440\n"
  "v_mfma_f32_16x16x32_bf16 v[184:187], v[32:35], v[104:107], v[184:187]\n"
  "ds_read_b64_tr_b16 v[30:31], v9 offset:63488\n"
  "s_waitcnt lgkmcnt(6)\n"
  "v_mfma_f32_16x16x32_bf16 v[176:179], v[36:39], v[112:115], v[176:179]\n"
  "v_mfma_f32_16x16x32_bf16 v[180:183], v[36:39], v[116:119], v[180:183]\n"
  "s_waitcnt lgkmcnt(4)\n"
  "v_mfma_f32_16x16x32_bf16 v[188:191], v[40:43], v[116:119], v[188:191]\n"
  "v_mfma_f32_16x16x32_bf16 v[184:187], v[40:43], v[112:115], v[184:187]\n"
  "s_waitcnt lgkmcnt(2)\n"
  "v_mfma_f32_16x16x32_bf16 v[176:179], v[44:47], v[120:123], v[176:179]\n"
  "v_mfma_f32_16x16x32_bf16 v[180:183], v[44:47], v[124:127], v[180:183]\n"
  "s_waitcnt lgkmcnt(0)\n"
  "v_mfma_f32_16x16x32_bf16 v[188:191], v[28:31], v[124:127], v[188:191]\n"
  "v_mfma_f32_16x16x32_bf16 v[184:187], v[28:31], v[120:123], v[184:187]\n"
  "s_mov_b64 exec, 1\n"
  "s_waitcnt vmcnt(0)\n"
  "buffer_store_dwordx2 v[26:27], v24, s[24:27], s71 offen sc1\n"
  "s_mov_b64 exec, -1\n"
  "buffer_load_dwordx2 v[48:49], v13, s[24:27], s70 offen sc1\n"
  "s_waitcnt vmcnt(0)\n"
  "v_cmp_ne_u64 vcc, s[48:49], v[48:49]\n"
  "s_cbranch_vccnz Lslow_p\n"
  "Lback_p:\n"
  "buffer_load_dwordx4 v[192:195], v1, s[24:27], s75 offen sc1\n"
  "buffer_load_dwordx4 v[196:199], v1, s[24:27], s75 offen offset:1024 sc1\n"
  "buffer_load_dwordx4 v[200:203], v1, s[24:27], s76 offen sc1\n"
  "buffer_load_dwordx4 v[204:207], v1, s[24:27], s76 offen offset:1024 sc1\n"
  "buffer_load_dwordx4 v[208:211], v1, s[24:27], s77 offen sc1\n"
  "buffer_load_dwordx4 v[212:215], v1, s[24:27], s77 offen offset:1024 sc1\n"
  "s_mov_b64 exec, 1\n"
  "buffer_store_dwordx2 v[22:23], v24, s[24:27], s79 offen sc1\n"
  "s_mov_b64 exec, -1\n"
  "s_waitcnt vmcnt(6)\n"
  "v_lshlrev_b32 v16, 16, v192\n"
  "v_and_b32 v17, 0xffff0000, v192\n"
  "v_pk_add_f32 v[176:177], v[176:177], v[16:17]\n"
  "v_lshlrev_b32 v18, 16, v193\n"
  "v_and_b32 v19, 0xffff0000, v193\n"
  "v_pk_add_f32 v[178:179], v[178:179], v[18:19]\n"
  "v_lshlrev_b32 v16, 16, v194\n"
  "v_and_b32 v17, 0xffff0000, v194\n"
  "v_pk_add_f32 v[184:185], v[184:185], v[16:17]\n"
  "v_lshlrev_b32 v18, 16, v195\n"
  "v_and_b32 v19, 0xffff0000, v195\n"
  "v_pk_add_f32 v[186:187], v[186:187], v[18:19]\n"
  "s_waitcnt vmcnt(5)\n"
  "v_lshlrev_b32 v16, 16, v196\n"
  "v_and_b32 v17, 0xffff0000, v196\n"
  "v_pk_add_f32 v[180:181], v[180:181], v[16:17]\n"
  "v_lshlrev_b32 v18, 16, v197\n"
  "v_and_b32 v19, 0xffff0000, v197\n"
  "v_pk_add_f32 v[182:183], v[182:183], v[18:19]\n"
  "v_lshlrev_b32 v16, 16, v198\n"
  "v_and_b32 v17, 0xffff0000, v198\n"
  "v_pk_add_f32 v[188:189], v[188:189], v[16:17]\n"
  "v_lshlrev_b32 v18, 16, v199\n"
  "v_and_b32 v19, 0xffff0000, v199\n"
  "v_pk_add_f32 v[190:191], v[190:191], v[18:19]\n"
  "s_waitcnt vmcnt(4)\n"
  "v_lshlrev_b32 v16, 16, v200\n"
  "v_and_b32 v17, 0xffff0000, v200\n"
  "v_pk_add_f32 v[176:177], v[176:177], v[16:17]\n"
  "v_lshlrev_b32 v18, 16, v201\n"
  "v_and_b32 v19, 0xffff0000, v201\n"
  "v_pk_add_f32 v[178:179], v[178:179], v[18:19]\n"
  "v_lshlrev_b32 v16, 16, v202\n"
  "v_and_b32 v17, 0xffff0000, v202\n"
  "v_pk_add_f32 v[184:185], v[184:185], v[16:17]\n"
  "v_lshlrev_b32 v18, 16, v203\n"
  "v_and_b32 v19, 0xffff0000, v203\n"
  "v_pk_add_f32 v[186:187], v[186:187], v[18:19]\n"
  "s_waitcnt vmcnt(3)\n"
  "v_lshlrev_b32 v16, 16, v204\n"
  "v_and_b32 v17, 0xffff0000, v204\n"
  "v_pk_add_f32 v[180:181], v[180:181], v[16:17]\n"
  "v_lshlrev_b32 v18, 16, v205\n"
  "v_and_b32 v19, 0xffff0000, v205\n"
  "v_pk_add_f32 v[182:183], v[182:183], v[18:19]\n"
  "v_lshlrev_b32 v16, 16, v206\n"
  "v_and_b32 v17, 0xffff0000, v206\n"
  "v_pk_add_f32 v[188:189], v[188:189], v[16:17]\n"
  "v_lshlrev_b32 v18, 16, v207\n"
  "v_and_b32 v19, 0xffff0000, v207\n"
  "v_pk_add_f32 v[190:191], v[190:191], v[18:19]\n"
  "s_waitcnt vmcnt(2)\n"
  "v_lshlrev_b32 v16, 16, v208\n"
  "v_and_b32 v17, 0xffff0000, v208\n"
  "v_pk_add_f32 v[176:177], v[176:177], v[16:17]\n"
  "v_lshlrev_b32 v18, 16, v209\n"
  "v_and_b32 v19, 0xffff0000, v209\n"
  "v_pk_add_f32 v[178:179], v[178:179], v[18:19]\n"
  "v_lshlrev_b32 v16, 16, v210\n"
  "v_and_b32 v17, 0xffff0000, v210\n"
  "v_pk_add_f32 v[184:185], v[184:185], v[16:17]\n"
  "v_lshlrev_b32 v18, 16, v211\n"
  "v_and_b32 v19, 0xffff0000, v211\n"
  "v_pk_add_f32 v[186:187], v[186:187], v[18:19]\n"
  "s_waitcnt vmcnt(1)\n"
  "v_lshlrev_b32 v16, 16, v212\n"
  "v_and_b32 v17, 0xffff0000, v212\n"
  "v_pk_add_f32 v[180:181], v[180:181], v[16:17]\n"
  "v_lshlrev_b32 v18, 16, v213\n"
  "v_and_b32 v19, 0xffff0000, v213\n"
  "v_pk_add_f32 v[182:183], v[182:183], v[18:19]\n"
  "v_lshlrev_b32 v16, 16, v214\n"
  "v_and_b32 v17, 0xffff0000, v214\n"
  "v_pk_add_f32 v[188:189], v[188:189], v[16:17]\n"
  "v_lshlrev_b32 v18, 16, v215\n"
  "v_and_b32 v19, 0xffff0000, v215\n"
  "v_pk_add_f32 v[190:191], v[190:191], v[18:19]\n"
  "v_cvt_pk_bf16_f32 v216, v176, v177\n"
  "v_cvt_pk_bf16_f32 v217, v178, v179\n"
  "v_cvt_pk_bf16_f32 v218, v184, v185\n"
  "v_cvt_pk_bf16_f32 v219, v186, v187\n"
  "s_nop 0\n"
  "v_permlane16_swap_b32 v216, v218\n"
  "v_permlane16_swap_b32 v217, v219\n"
  "buffer_store_dwordx4 v[216:219], v14, s[28:31], 0 offen sc1\n"
  "v_cvt_pk_bf16_f32 v220, v180, v181\n"
  "v_cvt_pk_bf16_f32 v221, v182, v183\n"
  "v_cvt_pk_bf16_f32 v222, v188, v189\n"
  "v_cvt_pk_bf16_f32 v223, v190, v191\n"
  "s_nop 0\n"
  "v_permlane16_swap_b32 v220, v222\n"
  "v_permlane16_swap_b32 v221, v223\n"
  "buffer_store_dwordx4 v[220:223], v14, s[28:31], s43 offen sc1\n"
  "s_endpgm\n"
  "Lslow_p:\n"
  "s_mov_b32 s81, 0x20000\n"
  "v_mov_b32 v50, v48\n"
  "v_mov_b32 v51, v49\n"
  "v_mov_b32 v52, v48\n"
  "v_mov_b32 v53, v49\n"
  "v_mov_b32 v54, v48\n"
  "v_mov_b32 v55, v49\n"
  "Lspin_p:\n"
  "buffer_load_dwordx2 v[48:49], v13, s[24:27], s70 offen sc1\n"
  "s_sleep 2\n"
  "s_waitcnt vmcnt(3)\n"
  "v_cmp_ne_u64 vcc, s[48:49], v[50:51]\n"
  "s_cbranch_vccz Lback_p\n"
  "buffer_load_dwordx2 v[50:51], v13, s[24:27], s70 offen sc1\n"
  "s_sleep 2\n"
  "s_waitcnt vmcnt(3)\n"
  "v_cmp_ne_u64 vcc, s[48:49], v[52:53]\n"
  "s_cbranch_vccz Lback_p\n"
  "buffer_load_dwordx2 v[52:53], v13, s[24:27], s70 offen sc1\n"
  "s_sleep 2\n"
  "s_waitcnt vmcnt(3)\n"
  "v_cmp_ne_u64 vcc, s[48:49], v[54:55]\n"
  "s_cbranch_vccz Lback_p\n"
  "buffer_load_dwordx2 v[54:55], v13, s[24:27], s70 offen sc1\n"
  "s_sleep 2\n"
  "s_waitcnt vmcnt(3)\n"
  "v_cmp_ne_u64 vcc, s[48:49], v[48:49]\n"
  "s_cbranch_vccz Lback_p\n"
  "s_sub_u32 s81, s81, 1\n"
  "s_cbranch_scc0 Lspin_p\n"
  "s_branch Lback_p\n"
  "Lslow_o0c0:\n"
  "s_mov_b32 s81, 0\n"
  "Lspin_o0c0:\n"
  "s_sleep 0\n"
  "buffer_load_dwordx2 v[24:25], v23, s[24:27], s68 offen sc1\n"
  "s_add_u32 s81, s81, 1\n"
  "s_cmp_gt_u32 s81, 0x40000\n"
  "s_waitcnt vmcnt(0)\n"
  "s_cbranch_scc1 Lback_o0c0\n"
  "v_cmp_ne_u64 vcc, s[46:47], v[24:25]\n"
  "s_cbranch_vccnz Lspin_o0c0\n"
  "s_branch Lback_o0c0\n"
  "Lslow_o1c0:\n"
  "s_mov_b32 s81, 0\n"
  "Lspin_o1c0:\n"
  "s_sleep 0\n"
  "buffer_load_dwordx2 v[24:25], v23, s[24:27], s68 offen offset:8 sc1\n"
  "s_add_u32 s81, s81, 1\n"
  "s_cmp_gt_u32 s81, 0x40000\n"
  "s_waitcnt vmcnt(0)\n"
  "s_cbranch_scc1 Lback_o1c0\n"
  "v_cmp_ne_u64 vcc, s[46:47], v[24:25]\n"
  "s_cbranch_vccnz Lspin_o1c0\n"
  "s_branch Lback_o1c0\n"
  "Lslow_o0c1:\n"
  "s_mov_b32 s81, 0\n"
  "Lspin_o0c1:\n"
  "s_sleep 0\n"
  "buffer_load_dwordx2 v[24:25], v23, s[24:27], s68 offen sc1\n"
  "s_add_u32 s81, s81, 1\n"
  "s_cmp_gt_u32 s81, 0x40000\n"
  "s_waitcnt vmcnt(0)\n"
  "s_cbranch_scc1 Lback_o0c1\n"
  "v_cmp_ne_u64 vcc, s[46:47], v[24:25]\n"
  "s_cbranch_vccnz Lspin_o0c1\n"
  "s_branch Lback_o0c1\n"
  "Lslow_o1c1:\n"
  "s_mov_b32 s81, 0\n"
  "Lspin_o1c1:\n"
  "s_sleep 0\n"
  "buffer_load_dwordx2 v[24:25], v23, s[24:27], s68 offen offset:8 sc1\n"
  "s_add_u32 s81, s81, 1\n"
  "s_cmp_gt_u32 s81, 0x40000\n"
  "s_waitcnt vmcnt(0)\n"
  "s_cbranch_scc1 Lback_o1c1\n"
  "v_cmp_ne_u64 vcc, s[46:47], v[24:25]\n"
  "s_cbranch_vccnz Lspin_o1c1\n"
  "s_branch Lback_o1c1\n"
  "Lcode1:\n"
  :
  : [tid] "v"(threadIdx.x), [ka] "s"(__builtin_amdgcn_kernarg_segment_ptr()), [wg] "s"(blockIdx.x)
  : "v1", "v2", "v3", "v4", "v5", "v6", "v7", "v8", "v9", "v10", "v11", "v12", "v13", "v14", "v15", "v16", "v17", "v18", "v19", "v20", "v21", "v22", "v23", "v24", "v25", "v26", "v27", "v28", "v29", "v30", "v31", "v32", "v33", "v34", "v35", "v36", "v37", "v38", "v39", "v40", "v41", "v42", "v43", "v44", "v45", "v46", "v47", "v48", "v49", "v50", "v51", "v52", "v53", "v54", "v55", "v56", "v57", "v58", "v59", "v60", "v61", "v62", "v63", "v64", "v65", "v66", "v67", "v68", "v69", "v70", "v71", "v72", "v73", "v74", "v75", "v76", "v77", "v78", "v79", "v80", "v81", "v82", "v83", "v84", "v85", "v86", "v87", "v88", "v89", "v90", "v91", "v92", "v93", "v94", "v95", "v96", "v97", "v98", "v99", "v100", "v101", "v102", "v103", "v104", "v105", "v106", "v107", "v108", "v109", "v110", "v111", "v112", "v113", "v114", "v115", "v116", "v117", "v118", "v119", "v120", "v121", "v122", "v123", "v124", "v125", "v126", "v127", "v128", "v129", "v130", "v131", "v132", "v133", "v134", "v135", "v136", "v137", "v138", "v139", "v140", "v141", "v142", "v143", "v144", "v145", "v146", "v147", "v148", "v149", "v150", "v151", "v152", "v153", "v154", "v155", "v156", "v157", "v158", "v159", "v160", "v161", "v162", "v163", "v164", "v165", "v166", "v167", "v168", "v169", "v170", "v171", "v172", "v173", "v174", "v175", "v176", "v177", "v178", "v179", "v180", "v181", "v182", "v183", "v184", "v185", "v186", "v187", "v188", "v189", "v190", "v191", "v192", "v193", "v194", "v195", "v196", "v197", "v198", "v199", "v200", "v201", "v202", "v203", "v204", "v205", "v206", "v207", "v208", "v209", "v210", "v211", "v212", "v213", "v214", "v215", "v216", "v217", "v218", "v219", "v220", "v221", "v222", "v223", "v224", "v225", "v226", "v227", "v228", "v229", "v230", "v231", "v232", "v233", "v234", "v235", "v236", "v237", "v238", "v239", "v240", "v241", "v242", "v243", "v244", "v245", "v246", "v247", "v248", "v249", "v250", "v251", "v252", "v253", "v254", "v255", "s4", "s5", "s6", "s7", "s8", "s9", "s10", "s11", "s12", "s13", "s14", "s15", "s16", "s17", "s18", "s19", "s20", "s21", "s22", "s23", "s24", "s25", "s26", "s27", "s28", "s29", "s30", "s31", "s33", "s34", "s35", "s36", "s37", "s38", "s39", "s40", "s41", "s42", "s43", "s44", "s45", "s46", "s47", "s48", "s49", "s50", "s51", "s52", "s53", "s54", "s55", "s56", "s57", "s58", "s59", "s60", "s61", "s62", "s63", "s64", "s65", "s66", "s67", "s68", "s69", "s70", "s71", "s72", "s73", "s74", "s75", "s76", "s77", "s78", "s79", "s80", "s81", "s82", "s83", "s84", "s85", "s86", "s87", "s88", "s89", "s90", "s91", "s92", "s93", "s94", "s95", "s96", "s97", "s98", "s99", "vcc", "scc", "memory");
}
extern "C" void kernel_launch(void* const* d_in, const int* in_sizes, int n_in, void* d_out, int out_size, void* d_ws, size_t ws_size, hipStream_t stream) {
  KArgs a;
  for (int i = 0; i < 10; ++i) a.in[i] = (const char*)d_in[i];
  const size_t used = (size_t)32 << 20;
  a.out = (char*)d_out; a.ws = (char*)d_ws + ((ws_size - used) & ~(size_t)0xfffff); a.ws_bytes = (unsigned)used; a.gap = 0;
  (void)hipFuncSetAttribute((const void*)fused_attn_block, hipFuncAttributeMaxDynamicSharedMemorySize, 163840);
  fused_attn_block<<<256, 512, 163840, stream>>>(a);
}
```
